```python
import torch

def _softmax(x, axis=-1):
    return torch.softmax(x, dim=axis)
def _gelu(x):
    return torch.nn.functional.gelu(x, approximate="tanh")
def _silu(x):
    return torch.nn.functional.silu(x)
def _sigmoid(x):
    return torch.sigmoid(x)
def _layernorm(x, axis=-1, eps=1e-5):
    m = x.mean(dim=axis, keepdim=True)
    v = x.var(dim=axis, keepdim=True, unbiased=False)
    return (x - m) / torch.sqrt(v + eps)
def _scatter_add(tgt, idx, src, axis=0):
    return tgt.clone().index_add_(axis, idx, src)
def _segment_sum(src, seg, n):
    out = torch.zeros((n,) + src.shape[1:], dtype=src.dtype)
    return out.index_add_(0, seg, src)
def _pad(x, pw):
    flat = tuple(v for pair in reversed(pw) for v in pair)
    return torch.nn.functional.pad(x, flat)

kernel_name = 'standalone__gqa_softmax__near_ridge'

def setup_inputs():

    _w = (lambda a: (lambda t: t.to(torch.bfloat16) if t.is_floating_point() else t)(torch.as_tensor(a)))
    B, Hq, Hkv, Sq, Sk, D = 32, 32, 4, 256, 256, 128
    return {
        "q": _w((lambda *s: torch.randn(*s, dtype=torch.bfloat16) * 0.8)(B, Hq, Sq, D)),
        "k": _w((lambda *s: torch.randn(*s, dtype=torch.bfloat16) * 0.8)(B, Hkv, Sk, D)),
        "v": _w((lambda *s: torch.randn(*s, dtype=torch.bfloat16) * 0.05)(B, Hkv, Sk, D)),
    }

def reference(q, k, v):
    B, Hq, Sq, D = q.shape
    Hkv = k.shape[1]
    rep = Hq // Hkv
    k = (lambda x, n, axis: torch.repeat_interleave(x, n, dim=axis))(k, rep, axis=1)
    v = (lambda x, n, axis: torch.repeat_interleave(x, n, dim=axis))(v, rep, axis=1)
    scale = D ** -0.5
    scores = torch.einsum("bhqd,bhkd->bhqk", q, k) * scale
    p = _softmax(scores, axis=-1)
    return torch.einsum("bhqk,bhkd->bhqd", p, v)
```

```cpp
#include <hip/hip_runtime.h>
#include <hip/hip_bf16.h>
typedef float f32x16 __attribute__((ext_vector_type(16)));
typedef float f32x2 __attribute__((ext_vector_type(2)));
typedef __bf16 bf16x8 __attribute__((ext_vector_type(8)));
typedef __bf16 bf16x2 __attribute__((ext_vector_type(2)));
typedef unsigned u32x4 __attribute__((ext_vector_type(4)));
typedef unsigned u32x2 __attribute__((ext_vector_type(2)));
typedef short s16x4 __attribute__((ext_vector_type(4)));
#define NW 4
constexpr int B = 32, HQ = 32, HKV = 4, SQ = 256, SK = 256, D = 128;
constexpr float C = 0.12751743082f;
__device__ __forceinline__ unsigned cvtpk(float lo, float hi) { f32x2 v = {lo, hi}; bf16x2 b = __builtin_convertvector(v, bf16x2); return __builtin_bit_cast(unsigned, b); }
__device__ __forceinline__ unsigned koff(unsigned row, unsigned ch) { return 256u * row + 16u * (ch ^ (row & 15u)); }
__device__ __forceinline__ unsigned voff(unsigned row, unsigned ch) { return 256u * row + 16u * (ch ^ (((row & 3u) << 2) | ((row >> 2) & 3u))); }

__global__ __launch_bounds__(64 * NW, NW / 4) void attn(const unsigned short* __restrict__ Q, const unsigned short* __restrict__ K, const unsigned short* __restrict__ V, unsigned short* __restrict__ O) {
  extern __shared__ __attribute__((aligned(16))) char lds[];
  char* Kl = lds; char* Vl = lds + 65536;
  const int tid = threadIdx.x, lane = tid & 63, wid = tid >> 6, r = lane & 31, h = lane >> 5;
  const int wg = blockIdx.x, xcd = wg & 7, idx = wg >> 3, pair = xcd * 16 + (idx >> 1), half = idx & 1;
  const int b = pair >> 2, kvh = pair & 3;
  const unsigned short* Kg = K + (long)pair * SK * D;
  const unsigned short* Vg = V + (long)pair * SK * D;
  for (int i = tid; i < 4096; i += 64 * NW) {
    const int row = i >> 4, ch = i & 15;
    u32x4 kv = *(const u32x4*)(Kg + row * D + ch * 8);
    *(u32x4*)(Kl + koff(row, ch)) = kv;
    u32x4 vv = *(const u32x4*)(Vg + row * D + ch * 8);
    unsigned w0 = __builtin_amdgcn_perm(vv[2], vv[0], 0x05040100u);
    unsigned w1 = __builtin_amdgcn_perm(vv[2], vv[0], 0x07060302u);
    unsigned w2 = __builtin_amdgcn_perm(vv[3], vv[1], 0x05040100u);
    unsigned w3 = __builtin_amdgcn_perm(vv[3], vv[1], 0x07060302u);
    unsigned w[4] = {w0, w1, w2, w3};
#pragma unroll
    for (int db = 0; db < 4; ++db) { const unsigned pos = 32 * db + 2 * ch; *(unsigned*)(Vl + voff(row, pos >> 3) + 2 * (pos & 7)) = w[db]; }
  }
  __syncthreads();
  const long qrow0 = ((long)(b * HQ + kvh * 8 + half * 4)) * SQ;
  const unsigned kbase = (unsigned)(size_t)Kl, vbase = (unsigned)(size_t)Vl;
  const unsigned g = lane >> 4, qq = (lane & 15) >> 2, p = lane & 3;
  for (int blk = wid; blk < 32; blk += NW) {
    const long row0 = qrow0 + blk * 32;
    bf16x8 qf[8];
#pragma unroll
    for (int s = 0; s < 8; ++s) qf[s] = *(const bf16x8*)(Q + (row0 + r) * D + 16 * s + 8 * h);
    f32x16 S[8];
#pragma unroll
    for (int kb = 0; kb < 8; ++kb) {
      f32x16 acc = {};
#pragma unroll
      for (int s = 0; s < 8; ++s) {
        bf16x8 kf = *(const bf16x8*)(Kl + koff(32 * kb + r, 2 * s + h));
        acc = __builtin_amdgcn_mfma_f32_32x32x16_bf16(kf, qf[s], acc, 0, 0, 0);
      }
      S[kb] = acc;
    }
    float m = S[0][0];
#pragma unroll
    for (int kb = 0; kb < 8; ++kb)
#pragma unroll
      for (int i = 0; i < 16; ++i) m = fmaxf(m, S[kb][i]);
    { auto rr = __builtin_amdgcn_permlane32_swap(__float_as_uint(m), __float_as_uint(m), false, false); m = fmaxf(__uint_as_float(rr[0]), __uint_as_float(rr[1])); }
    const float mc = -m * C;
    float l = 0.f;
    bf16x8 P[8][2];
#pragma unroll
    for (int kb = 0; kb < 8; ++kb) {
#pragma unroll
      for (int i = 0; i < 16; ++i) { S[kb][i] = __builtin_amdgcn_exp2f(fmaf(S[kb][i], C, mc)); l += S[kb][i]; }
#pragma unroll
      for (int t = 0; t < 2; ++t) { u32x4 w; for (int jj = 0; jj < 4; ++jj) w[jj] = cvtpk(S[kb][8 * t + 2 * jj], S[kb][8 * t + 2 * jj + 1]); P[kb][t] = __builtin_bit_cast(bf16x8, w); }
    }
    { auto rr = __builtin_amdgcn_permlane32_swap(__float_as_uint(l), __float_as_uint(l), false, false); l = __uint_as_float(rr[0]) + __uint_as_float(rr[1]); }
    f32x16 Oa[4] = {};
#pragma unroll
    for (int kb = 0; kb < 8; ++kb)
#pragma unroll
      for (int t = 0; t < 2; ++t)
#pragma unroll
        for (int db = 0; db < 4; ++db) {
          s16x4 lo, hi;
          const unsigned k0 = 32 * kb + 16 * t + 4 * h + qq;
          const unsigned pos = 32 * db + 16 * (g & 1) + 4 * p;
          const unsigned a0 = vbase + voff(k0, pos >> 3) + 2 * (pos & 7), a1 = vbase + voff(k0 + 8, pos >> 3) + 2 * (pos & 7);
          asm volatile("ds_read_b64_tr_b16 %0, %2\n\tds_read_b64_tr_b16 %1, %3\n\ts_waitcnt lgkmcnt(0)" : "=&v"(lo), "=&v"(hi) : "v"(a0), "v"(a1) : "memory");
          bf16x8 vf = __builtin_bit_cast(bf16x8, (__attribute__((ext_vector_type(8))) short){lo[0], lo[1], lo[2], lo[3], hi[0], hi[1], hi[2], hi[3]});
          Oa[db] = __builtin_amdgcn_mfma_f32_32x32x16_bf16(P[kb][t], vf, Oa[db], 0, 0, 0);
        }
    const float rl = __builtin_amdgcn_rcpf(l);
    f32x16 L = {};
    L = __builtin_amdgcn_mfma_f32_32x32x2f32(h == 0 ? rl : 0.f, 1.0f, L, 0, 0, 0);
#pragma unroll
    for (int i = 0; i < 16; ++i) {
      const int qrow = (i & 3) + 8 * (i >> 2) + 4 * h;
      u32x2 w = {cvtpk(Oa[0][i] * L[i], Oa[1][i] * L[i]), cvtpk(Oa[2][i] * L[i], Oa[3][i] * L[i])};
      *(u32x2*)(O + (row0 + qrow) * D + 4 * r) = w;
    }
  }
}
extern "C" void kernel_launch(void* const* d_in, const int* in_sizes, int n_in, void* d_out, int out_size, void* d_ws, size_t ws_size, hipStream_t stream) {
  hipFuncSetAttribute((const void*)attn, hipFuncAttributeMaxDynamicSharedMemorySize, 131072);
  attn<<<256, 64 * NW, 131072, stream>>>((const unsigned short*)d_in[0], (const unsigned short*)d_in[1], (const unsigned short*)d_in[2], (unsigned short*)d_out);
}
```

```cpp
#include <hip/hip_runtime.h>
#include <hip/hip_bf16.h>

__global__ __launch_bounds__(512, 2) void gqa_attn_kernel(const char* __restrict__ Q, const char* __restrict__ K,
                                                          const char* __restrict__ V, char* __restrict__ O) {
  __shared__ __attribute__((aligned(16))) char lds[163840];
  const unsigned wid = __builtin_amdgcn_readfirstlane(threadIdx.x >> 6);
  const unsigned ldsb = (unsigned)(size_t)lds;
  auto ka = __builtin_amdgcn_kernarg_segment_ptr();
  asm volatile(
      "s_load_dwordx8 s[40:47], %0, 0\n"
      "s_mov_b32 s49, %1\n"
      "s_and_b32 s64, %2, 7\n"
      "s_lshr_b32 s65, %2, 3\n"
      "s_lshl_b32 s64, s64, 4\n"
      "s_lshr_b32 s66, s65, 1\n"
      "s_add_u32 s64, s64, s66\n"
      "s_and_b32 s65, s65, 1\n"
      "s_lshl_b32 s66, s64, 1\n"
      "s_add_u32 s66, s66, s65\n"
      "s_lshl_b32 s66, s66, 18\n"
      "s_lshl_b32 s67, s49, 13\n"
      "s_add_u32 s67, s67, 4096\n"
      "s_add_u32 s66, s66, s67\n"
      "s_lshl_b32 s64, s64, 16\n"
      "s_lshl_b32 s75, s65, 13\n"
      "s_and_b32 s74, s49, 3\n"
      "s_lshl_b32 s74, s74, 14\n"
      "s_add_u32 s74, s74, s75\n"
      "s_add_u32 s64, s64, s74\n"
      "s_add_u32 s64, s64, 4096\n"
      "s_lshl_b32 s71, s75, 1\n"
      "s_sub_u32 s71, 8192, s71\n"
      "v_mbcnt_lo_u32_b32 v217, -1, 0\n"
      "v_mbcnt_hi_u32_b32 v217, -1, v217\n"
      "v_lshlrev_b32 v211, 4, v217\n"
      "v_lshrrev_b32 v218, 5, v217\n"
      "v_and_b32 v219, 31, v217\n"
      "v_lshlrev_b32 v214, 10, v218\n"
      "v_and_b32 v220, 15, v219\n"
      "v_lshl_add_u32 v214, v220, 4, v214\n"
      "v_lshrrev_b32 v220, 4, v219\n"
      "v_lshl_add_u32 v214, v220, 3, v214\n"
      "s_waitcnt lgkmcnt(0)\n"
      "s_mov_b64 s[68:69], s[46:47]\n"
      "s_add_u32 s46, s44, s64\n"
      "s_addc_u32 s47, s45, 0\n"
      "s_add_u32 s44, s42, s64\n"
      "s_addc_u32 s45, s43, 0\n"
      "s_add_u32 s42, s68, s66\n"
      "s_addc_u32 s43, s69, 0\n"
      "s_add_u32 s40, s40, s66\n"
      "s_addc_u32 s41, s41, 0\n"
      "v_mov_b32 v220, 159744\n"
      "v_mov_b32 v221, 0\n"
      "ds_write_b32 v220, v221\n"
      "s_cmp_ge_u32 s49, 4\n"
      "s_cbranch_scc1 .Lnoq0_%=\n"
      "global_load_dwordx4 v[0:3], v211, s[44:45] offset:-4096\n"
      "global_load_dwordx4 v[4:7], v211, s[44:45] offset:-3072\n"
      "global_load_dwordx4 v[8:11], v211, s[44:45] offset:-2048\n"
      "global_load_dwordx4 v[12:15], v211, s[44:45] offset:-1024\n"
      "global_load_dwordx4 v[16:19], v211, s[44:45] offset:0\n"
      "global_load_dwordx4 v[20:23], v211, s[44:45] offset:1024\n"
      "global_load_dwordx4 v[24:27], v211, s[44:45] offset:2048\n"
      "global_load_dwordx4 v[28:31], v211, s[44:45] offset:3072\n"
      "s_ashr_i32 s64, s71, 31\n"
      "s_add_u32 s44, s44, s71\n"
      "s_addc_u32 s45, s45, s64\n"
      "global_load_dwordx4 v[96:99], v211, s[40:41] offset:-4096 nt\n"
      "global_load_dwordx4 v[100:103], v211, s[40:41] offset:-3072 nt\n"
      "global_load_dwordx4 v[104:107], v211, s[40:41] offset:-2048 nt\n"
      "global_load_dwordx4 v[108:111], v211, s[40:41] offset:-1024 nt\n"
      "global_load_dwordx4 v[112:115], v211, s[40:41] offset:0 nt\n"
      "global_load_dwordx4 v[116:119], v211, s[40:41] offset:1024 nt\n"
      "global_load_dwordx4 v[120:123], v211, s[40:41] offset:2048 nt\n"
      "global_load_dwordx4 v[124:127], v211, s[40:41] offset:3072 nt\n"
      "global_load_dwordx4 v[32:35], v211, s[44:45] offset:-4096\n"
      "global_load_dwordx4 v[36:39], v211, s[44:45] offset:-3072\n"
      "global_load_dwordx4 v[40:43], v211, s[44:45] offset:-2048\n"
      "global_load_dwordx4 v[44:47], v211, s[44:45] offset:-1024\n"
      "global_load_dwordx4 v[48:51], v211, s[44:45] offset:0\n"
      "global_load_dwordx4 v[52:55], v211, s[44:45] offset:1024\n"
      "global_load_dwordx4 v[56:59], v211, s[44:45] offset:2048\n"
      "global_load_dwordx4 v[60:63], v211, s[44:45] offset:3072\n"
      ".Lnoq0_%=:\n"
      "s_mov_b32 s50, 0x3e0293ee\n"
      "s_mov_b32 s51, 0x3e0293ee\n"
      "s_mov_b32 s52, 0x05040100\n"
      "s_mov_b32 s53, 0x07060302\n"
      "s_mov_b32 s54, 0xffff0000\n"
      "s_mov_b32 s55, 0x7fff7fff\n"
      "s_mov_b32 s56, 0x2b800000\n"
      "s_mov_b32 s57, 0x5d800000\n"
      "s_mov_b32 s58, 0x0000ffff\n"
      "s_mov_b32 s59, 0x0000ffff\n"
      "s_mov_b32 s60, 0xffff0000\n"
      "s_mov_b32 s61, 0xffff0000\n"
      "s_lshl_b32 s63, s49, 12\n"
      "s_add_u32 s63, s63, 131072\n"
      "s_mov_b32 s48, 4\n"
      "s_mov_b32 s62, 0\n"
      "s_movk_i32 s72, 0x2180\n"
      "v_lshrrev_b32 v220, 3, v219\n"
      "v_lshlrev_b32 v220, 11, v220\n"
      "v_and_b32 v221, 7, v219\n"
      "v_lshl_add_u32 v220, v221, 6, v220\n"
      "v_bfe_u32 v221, v219, 2, 2\n"
      "v_xor_b32 v221, v221, v218\n"
      "v_lshl_add_u32 v208, v221, 4, v220\n"
      "v_xor_b32 v209, 32, v208\n"
      "v_add_u32 v210, 0x10000, v211\n"
      "v_lshrrev_b32 v220, 4, v217\n"
      "v_and_b32 v221, 15, v217\n"
      "v_xor_b32 v222, v221, v220\n"
      "v_lshlrev_b32 v222, 4, v222\n"
      "v_lshl_add_u32 v222, v220, 8, v222\n"
      "v_add_u32 v212, s63, v222\n"
      "v_lshrrev_b32 v222, 2, v221\n"
      "v_lshlrev_b32 v222, 9, v222\n"
      "v_lshl_add_u32 v222, v220, 6, v222\n"
      "v_and_b32 v221, 3, v221\n"
      "v_lshl_add_u32 v222, v221, 4, v222\n"
      "v_add_u32 v235, s74, v222\n"
      "v_and_b32 v220, 15, v219\n"
      "v_and_b32 v221, 2, v220\n"
      "v_and_b32 v222, 4, v220\n"
      "v_and_b32 v220, 9, v220\n"
      "v_lshl_or_b32 v220, v221, 1, v220\n"
      "v_lshrrev_b32 v222, 1, v222\n"
      "v_or_b32 v220, v220, v222\n"
      "v_xor_b32 v221, v220, v218\n"
      "v_lshlrev_b32 v221, 4, v221\n"
      "v_lshl_add_u32 v221, v220, 8, v221\n"
      "v_add_u32 v213, s63, v221\n"
      "v_lshlrev_b32 v220, 2, v219\n"
      "v_add_u32 v228, s63, v220\n"
      "v_lshlrev_b32 v220, 4, v218\n"
      "v_add_u32 v229, s63, v220\n"
      "s_waitcnt lgkmcnt(0)\n"
      "s_barrier\n"
      "s_cmp_lt_u32 s49, 4\n"
      "s_cbranch_scc1 .Lnov_%=\n"
      "global_load_dwordx2 v[32:33], v214, s[46:47] offset:-4096\n"
      "global_load_dwordx2 v[34:35], v214, s[46:47] offset:-3840\n"
      "global_load_dwordx2 v[36:37], v214, s[46:47] offset:-3584\n"
      "global_load_dwordx2 v[38:39], v214, s[46:47] offset:-3328\n"
      "global_load_dwordx2 v[40:41], v214, s[46:47] offset:-2048\n"
      "global_load_dwordx2 v[42:43], v214, s[46:47] offset:-1792\n"
      "global_load_dwordx2 v[44:45], v214, s[46:47] offset:-1536\n"
      "global_load_dwordx2 v[46:47], v214, s[46:47] offset:-1280\n"
      "global_load_dwordx2 v[48:49], v214, s[46:47] offset:0\n"
      "global_load_dwordx2 v[50:51], v214, s[46:47] offset:256\n"
      "global_load_dwordx2 v[52:53], v214, s[46:47] offset:512\n"
      "global_load_dwordx2 v[54:55], v214, s[46:47] offset:768\n"
      "global_load_dwordx2 v[56:57], v214, s[46:47] offset:2048\n"
      "global_load_dwordx2 v[58:59], v214, s[46:47] offset:2304\n"
      "global_load_dwordx2 v[60:61], v214, s[46:47] offset:2560\n"
      "global_load_dwordx2 v[62:63], v214, s[46:47] offset:2816\n"
      "s_ashr_i32 s64, s71, 31\n"
      "s_add_u32 s46, s46, s71\n"
      "s_addc_u32 s47, s47, s64\n"
      "global_load_dwordx4 v[96:99], v211, s[40:41] offset:-4096 nt\n"
      "global_load_dwordx4 v[100:103], v211, s[40:41] offset:-3072 nt\n"
      "global_load_dwordx4 v[104:107], v211, s[40:41] offset:-2048 nt\n"
      "global_load_dwordx4 v[108:111], v211, s[40:41] offset:-1024 nt\n"
      "global_load_dwordx4 v[112:115], v211, s[40:41] offset:0 nt\n"
      "global_load_dwordx4 v[116:119], v211, s[40:41] offset:1024 nt\n"
      "global_load_dwordx4 v[120:123], v211, s[40:41] offset:2048 nt\n"
      "global_load_dwordx4 v[124:127], v211, s[40:41] offset:3072 nt\n"
      "global_load_dwordx2 v[128:129], v214, s[46:47] offset:-4096\n"
      "global_load_dwordx2 v[130:131], v214, s[46:47] offset:-3840\n"
      "global_load_dwordx2 v[132:133], v214, s[46:47] offset:-3584\n"
      "global_load_dwordx2 v[134:135], v214, s[46:47] offset:-3328\n"
      "global_load_dwordx2 v[136:137], v214, s[46:47] offset:-2048\n"
      "global_load_dwordx2 v[138:139], v214, s[46:47] offset:-1792\n"
      "global_load_dwordx2 v[140:141], v214, s[46:47] offset:-1536\n"
      "global_load_dwordx2 v[142:143], v214, s[46:47] offset:-1280\n"
      "global_load_dwordx2 v[144:145], v214, s[46:47] offset:0\n"
      "global_load_dwordx2 v[146:147], v214, s[46:47] offset:256\n"
      "global_load_dwordx2 v[148:149], v214, s[46:47] offset:512\n"
      "global_load_dwordx2 v[150:151], v214, s[46:47] offset:768\n"
      "global_load_dwordx2 v[152:153], v214, s[46:47] offset:2048\n"
      "global_load_dwordx2 v[154:155], v214, s[46:47] offset:2304\n"
      "global_load_dwordx2 v[156:157], v214, s[46:47] offset:2560\n"
      "global_load_dwordx2 v[158:159], v214, s[46:47] offset:2816\n"
      ".Lnov_%=:\n"
      "s_cmp_ge_u32 s49, 4\n"
      "s_cbranch_scc1 .LkB_%=\n"
      "s_waitcnt vmcnt(23)\n"
      "v_lshlrev_b32 v176, 16, v0\n"
      "v_and_b32 v177, s54, v0\n"
      "v_pk_mul_f32 v[176:177], v[176:177], s[50:51]\n"
      "v_cvt_pk_bf16_f32 v0, v176, v177\n"
      "v_lshlrev_b32 v178, 16, v1\n"
      "v_and_b32 v179, s54, v1\n"
      "v_pk_mul_f32 v[178:179], v[178:179], s[50:51]\n"
      "v_cvt_pk_bf16_f32 v1, v178, v179\n"
      "v_lshlrev_b32 v180, 16, v2\n"
      "v_and_b32 v181, s54, v2\n"
      "v_pk_mul_f32 v[180:181], v[180:181], s[50:51]\n"
      "v_cvt_pk_bf16_f32 v2, v180, v181\n"
      "v_lshlrev_b32 v182, 16, v3\n"
      "v_and_b32 v183, s54, v3\n"
      "v_pk_mul_f32 v[182:183], v[182:183], s[50:51]\n"
      "v_cvt_pk_bf16_f32 v3, v182, v183\n"
      "v_xor_b32 v192, 0, v235\n"
      "ds_write_b128 v192, v[0:3] offset:0\n"
      "s_waitcnt vmcnt(22)\n"
      "v_lshlrev_b32 v184, 16, v4\n"
      "v_and_b32 v185, s54, v4\n"
      "v_pk_mul_f32 v[184:185], v[184:185], s[50:51]\n"
      "v_cvt_pk_bf16_f32 v4, v184, v185\n"
      "v_lshlrev_b32 v186, 16, v5\n"
      "v_and_b32 v187, s54, v5\n"
      "v_pk_mul_f32 v[186:187], v[186:187], s[50:51]\n"
      "v_cvt_pk_bf16_f32 v5, v186, v187\n"
      "v_lshlrev_b32 v188, 16, v6\n"
      "v_and_b32 v189, s54, v6\n"
      "v_pk_mul_f32 v[188:189], v[188:189], s[50:51]\n"
      "v_cvt_pk_bf16_f32 v6, v188, v189\n"
      "v_lshlrev_b32 v190, 16, v7\n"
      "v_and_b32 v191, s54, v7\n"
      "v_pk_mul_f32 v[190:191], v[190:191], s[50:51]\n"
      "v_cvt_pk_bf16_f32 v7, v190, v191\n"
      "v_xor_b32 v193, 16, v235\n"
      "ds_write_b128 v193, v[4:7] offset:256\n"
      "s_waitcnt vmcnt(21)\n"
      "v_lshlrev_b32 v176, 16, v8\n"
      "v_and_b32 v177, s54, v8\n"
      "v_pk_mul_f32 v[176:177], v[176:177], s[50:51]\n"
      "v_cvt_pk_bf16_f32 v8, v176, v177\n"
      "v_lshlrev_b32 v178, 16, v9\n"
      "v_and_b32 v179, s54, v9\n"
      "v_pk_mul_f32 v[178:179], v[178:179], s[50:51]\n"
      "v_cvt_pk_bf16_f32 v9, v178, v179\n"
      "v_lshlrev_b32 v180, 16, v10\n"
      "v_and_b32 v181, s54, v10\n"
      "v_pk_mul_f32 v[180:181], v[180:181], s[50:51]\n"
      "v_cvt_pk_bf16_f32 v10, v180, v181\n"
      "v_lshlrev_b32 v182, 16, v11\n"
      "v_and_b32 v183, s54, v11\n"
      "v_pk_mul_f32 v[182:183], v[182:183], s[50:51]\n"
      "v_cvt_pk_bf16_f32 v11, v182, v183\n"
      "v_xor_b32 v194, 32, v235\n"
      "ds_write_b128 v194, v[8:11] offset:2048\n"
      "s_waitcnt vmcnt(20)\n"
      "v_lshlrev_b32 v184, 16, v12\n"
      "v_and_b32 v185, s54, v12\n"
      "v_pk_mul_f32 v[184:185], v[184:185], s[50:51]\n"
      "v_cvt_pk_bf16_f32 v12, v184, v185\n"
      "v_lshlrev_b32 v186, 16, v13\n"
      "v_and_b32 v187, s54, v13\n"
      "v_pk_mul_f32 v[186:187], v[186:187], s[50:51]\n"
      "v_cvt_pk_bf16_f32 v13, v186, v187\n"
      "v_lshlrev_b32 v188, 16, v14\n"
      "v_and_b32 v189, s54, v14\n"
      "v_pk_mul_f32 v[188:189], v[188:189], s[50:51]\n"
      "v_cvt_pk_bf16_f32 v14, v188, v189\n"
      "v_lshlrev_b32 v190, 16, v15\n"
      "v_and_b32 v191, s54, v15\n"
      "v_pk_mul_f32 v[190:191], v[190:191], s[50:51]\n"
      "v_cvt_pk_bf16_f32 v15, v190, v191\n"
      "v_xor_b32 v195, 48, v235\n"
      "ds_write_b128 v195, v[12:15] offset:2304\n"
      "s_waitcnt vmcnt(19)\n"
      "v_lshlrev_b32 v176, 16, v16\n"
      "v_and_b32 v177, s54, v16\n"
      "v_pk_mul_f32 v[176:177], v[176:177], s[50:51]\n"
      "v_cvt_pk_bf16_f32 v16, v176, v177\n"
      "v_lshlrev_b32 v178, 16, v17\n"
      "v_and_b32 v179, s54, v17\n"
      "v_pk_mul_f32 v[178:179], v[178:179], s[50:51]\n"
      "v_cvt_pk_bf16_f32 v17, v178, v179\n"
      "v_lshlrev_b32 v180, 16, v18\n"
      "v_and_b32 v181, s54, v18\n"
      "v_pk_mul_f32 v[180:181], v[180:181], s[50:51]\n"
      "v_cvt_pk_bf16_f32 v18, v180, v181\n"
      "v_lshlrev_b32 v182, 16, v19\n"
      "v_and_b32 v183, s54, v19\n"
      "v_pk_mul_f32 v[182:183], v[182:183], s[50:51]\n"
      "v_cvt_pk_bf16_f32 v19, v182, v183\n"
      "v_xor_b32 v192, 0, v235\n"
      "ds_write_b128 v192, v[16:19] offset:4096\n"
      "s_waitcnt vmcnt(18)\n"
      "v_lshlrev_b32 v184, 16, v20\n"
      "v_and_b32 v185, s54, v20\n"
      "v_pk_mul_f32 v[184:185], v[184:185], s[50:51]\n"
      "v_cvt_pk_bf16_f32 v20, v184, v185\n"
      "v_lshlrev_b32 v186, 16, v21\n"
      "v_and_b32 v187, s54, v21\n"
      "v_pk_mul_f32 v[186:187], v[186:187], s[50:51]\n"
      "v_cvt_pk_bf16_f32 v21, v186, v187\n"
      "v_lshlrev_b32 v188, 16, v22\n"
      "v_and_b32 v189, s54, v22\n"
      "v_pk_mul_f32 v[188:189], v[188:189], s[50:51]\n"
      "v_cvt_pk_bf16_f32 v22, v188, v189\n"
      "v_lshlrev_b32 v190, 16, v23\n"
      "v_and_b32 v191, s54, v23\n"
      "v_pk_mul_f32 v[190:191], v[190:191], s[50:51]\n"
      "v_cvt_pk_bf16_f32 v23, v190, v191\n"
      "v_xor_b32 v193, 16, v235\n"
      "ds_write_b128 v193, v[20:23] offset:4352\n"
      "s_waitcnt vmcnt(17)\n"
      "v_lshlrev_b32 v176, 16, v24\n"
      "v_and_b32 v177, s54, v24\n"
      "v_pk_mul_f32 v[176:177], v[176:177], s[50:51]\n"
      "v_cvt_pk_bf16_f32 v24, v176, v177\n"
      "v_lshlrev_b32 v178, 16, v25\n"
      "v_and_b32 v179, s54, v25\n"
      "v_pk_mul_f32 v[178:179], v[178:179], s[50:51]\n"
      "v_cvt_pk_bf16_f32 v25, v178, v179\n"
      "v_lshlrev_b32 v180, 16, v26\n"
      "v_and_b32 v181, s54, v26\n"
      "v_pk_mul_f32 v[180:181], v[180:181], s[50:51]\n"
      "v_cvt_pk_bf16_f32 v26, v180, v181\n"
      "v_lshlrev_b32 v182, 16, v27\n"
      "v_and_b32 v183, s54, v27\n"
      "v_pk_mul_f32 v[182:183], v[182:183], s[50:51]\n"
      "v_cvt_pk_bf16_f32 v27, v182, v183\n"
      "v_xor_b32 v194, 32, v235\n"
      "ds_write_b128 v194, v[24:27] offset:6144\n"
      "s_waitcnt vmcnt(16)\n"
      "v_lshlrev_b32 v184, 16, v28\n"
      "v_and_b32 v185, s54, v28\n"
      "v_pk_mul_f32 v[184:185], v[184:185], s[50:51]\n"
      "v_cvt_pk_bf16_f32 v28, v184, v185\n"
      "v_lshlrev_b32 v186, 16, v29\n"
      "v_and_b32 v187, s54, v29\n"
      "v_pk_mul_f32 v[186:187], v[186:187], s[50:51]\n"
      "v_cvt_pk_bf16_f32 v29, v186, v187\n"
      "v_lshlrev_b32 v188, 16, v30\n"
      "v_and_b32 v189, s54, v30\n"
      "v_pk_mul_f32 v[188:189], v[188:189], s[50:51]\n"
      "v_cvt_pk_bf16_f32 v30, v188, v189\n"
      "v_lshlrev_b32 v190, 16, v31\n"
      "v_and_b32 v191, s54, v31\n"
      "v_pk_mul_f32 v[190:191], v[190:191], s[50:51]\n"
      "v_cvt_pk_bf16_f32 v31, v190, v191\n"
      "v_xor_b32 v195, 48, v235\n"
      "ds_write_b128 v195, v[28:31] offset:6400\n"
      "v_add_u32 v235, s71, v235\n"
      "s_waitcnt vmcnt(8)\n"
      "ds_write_b128 v212, v[96:99] offset:0\n"
      "v_xor_b32 v218, 64, v212\n"
      "ds_write_b128 v218, v[100:103] offset:1024\n"
      "v_xor_b32 v219, 128, v212\n"
      "ds_write_b128 v219, v[104:107] offset:2048\n"
      "v_xor_b32 v220, 192, v212\n"
      "ds_write_b128 v220, v[108:111] offset:3072\n"
      "s_mov_b64 exec, s[58:59]\n"
      "ds_read_b128 v[64:67], v213\n"
      "v_xor_b32 v222, 32, v213\n"
      "ds_read_b128 v[68:71], v222\n"
      "v_xor_b32 v223, 64, v213\n"
      "ds_read_b128 v[72:75], v223\n"
      "v_xor_b32 v224, 96, v213\n"
      "ds_read_b128 v[76:79], v224\n"
      "v_xor_b32 v221, 128, v213\n"
      "ds_read_b128 v[80:83], v221\n"
      "v_xor_b32 v222, 160, v213\n"
      "ds_read_b128 v[84:87], v222\n"
      "v_xor_b32 v223, 192, v213\n"
      "ds_read_b128 v[88:91], v223\n"
      "v_xor_b32 v224, 224, v213\n"
      "ds_read_b128 v[92:95], v224\n"
      "s_mov_b64 exec, -1\n"
      "ds_write_b128 v212, v[112:115] offset:0\n"
      "v_xor_b32 v218, 64, v212\n"
      "ds_write_b128 v218, v[116:119] offset:1024\n"
      "v_xor_b32 v219, 128, v212\n"
      "ds_write_b128 v219, v[120:123] offset:2048\n"
      "v_xor_b32 v220, 192, v212\n"
      "ds_write_b128 v220, v[124:127] offset:3072\n"
      "s_mov_b64 exec, s[60:61]\n"
      "ds_read_b128 v[64:67], v213\n"
      "v_xor_b32 v222, 32, v213\n"
      "ds_read_b128 v[68:71], v222\n"
      "v_xor_b32 v223, 64, v213\n"
      "ds_read_b128 v[72:75], v223\n"
      "v_xor_b32 v224, 96, v213\n"
      "ds_read_b128 v[76:79], v224\n"
      "v_xor_b32 v221, 128, v213\n"
      "ds_read_b128 v[80:83], v221\n"
      "v_xor_b32 v222, 160, v213\n"
      "ds_read_b128 v[84:87], v222\n"
      "v_xor_b32 v223, 192, v213\n"
      "ds_read_b128 v[88:91], v223\n"
      "v_xor_b32 v224, 224, v213\n"
      "ds_read_b128 v[92:95], v224\n"
      "s_mov_b64 exec, -1\n"
      "s_waitcnt vmcnt(7)\n"
      "v_lshlrev_b32 v176, 16, v32\n"
      "v_and_b32 v177, s54, v32\n"
      "v_pk_mul_f32 v[176:177], v[176:177], s[50:51]\n"
      "v_cvt_pk_bf16_f32 v32, v176, v177\n"
      "v_lshlrev_b32 v178, 16, v33\n"
      "v_and_b32 v179, s54, v33\n"
      "v_pk_mul_f32 v[178:179], v[178:179], s[50:51]\n"
      "v_cvt_pk_bf16_f32 v33, v178, v179\n"
      "v_lshlrev_b32 v180, 16, v34\n"
      "v_and_b32 v181, s54, v34\n"
      "v_pk_mul_f32 v[180:181], v[180:181], s[50:51]\n"
      "v_cvt_pk_bf16_f32 v34, v180, v181\n"
      "v_lshlrev_b32 v182, 16, v35\n"
      "v_and_b32 v183, s54, v35\n"
      "v_pk_mul_f32 v[182:183], v[182:183], s[50:51]\n"
      "v_cvt_pk_bf16_f32 v35, v182, v183\n"
      "v_xor_b32 v192, 0, v235\n"
      "ds_write_b128 v192, v[32:35] offset:0\n"
      "s_waitcnt vmcnt(6)\n"
      "v_lshlrev_b32 v184, 16, v36\n"
      "v_and_b32 v185, s54, v36\n"
      "v_pk_mul_f32 v[184:185], v[184:185], s[50:51]\n"
      "v_cvt_pk_bf16_f32 v36, v184, v185\n"
      "v_lshlrev_b32 v186, 16, v37\n"
      "v_and_b32 v187, s54, v37\n"
      "v_pk_mul_f32 v[186:187], v[186:187], s[50:51]\n"
      "v_cvt_pk_bf16_f32 v37, v186, v187\n"
      "v_lshlrev_b32 v188, 16, v38\n"
      "v_and_b32 v189, s54, v38\n"
      "v_pk_mul_f32 v[188:189], v[188:189], s[50:51]\n"
      "v_cvt_pk_bf16_f32 v38, v188, v189\n"
      "v_lshlrev_b32 v190, 16, v39\n"
      "v_and_b32 v191, s54, v39\n"
      "v_pk_mul_f32 v[190:191], v[190:191], s[50:51]\n"
      "v_cvt_pk_bf16_f32 v39, v190, v191\n"
      "v_xor_b32 v193, 16, v235\n"
      "ds_write_b128 v193, v[36:39] offset:256\n"
      "s_waitcnt vmcnt(5)\n"
      "v_lshlrev_b32 v176, 16, v40\n"
      "v_and_b32 v177, s54, v40\n"
      "v_pk_mul_f32 v[176:177], v[176:177], s[50:51]\n"
      "v_cvt_pk_bf16_f32 v40, v176, v177\n"
      "v_lshlrev_b32 v178, 16, v41\n"
      "v_and_b32 v179, s54, v41\n"
      "v_pk_mul_f32 v[178:179], v[178:179], s[50:51]\n"
      "v_cvt_pk_bf16_f32 v41, v178, v179\n"
      "v_lshlrev_b32 v180, 16, v42\n"
      "v_and_b32 v181, s54, v42\n"
      "v_pk_mul_f32 v[180:181], v[180:181], s[50:51]\n"
      "v_cvt_pk_bf16_f32 v42, v180, v181\n"
      "v_lshlrev_b32 v182, 16, v43\n"
      "v_and_b32 v183, s54, v43\n"
      "v_pk_mul_f32 v[182:183], v[182:183], s[50:51]\n"
      "v_cvt_pk_bf16_f32 v43, v182, v183\n"
      "v_xor_b32 v194, 32, v235\n"
      "ds_write_b128 v194, v[40:43] offset:2048\n"
      "s_waitcnt vmcnt(4)\n"
      "v_lshlrev_b32 v184, 16, v44\n"
      "v_and_b32 v185, s54, v44\n"
      "v_pk_mul_f32 v[184:185], v[184:185], s[50:51]\n"
      "v_cvt_pk_bf16_f32 v44, v184, v185\n"
      "v_lshlrev_b32 v186, 16, v45\n"
      "v_and_b32 v187, s54, v45\n"
      "v_pk_mul_f32 v[186:187], v[186:187], s[50:51]\n"
      "v_cvt_pk_bf16_f32 v45, v186, v187\n"
      "v_lshlrev_b32 v188, 16, v46\n"
      "v_and_b32 v189, s54, v46\n"
      "v_pk_mul_f32 v[188:189], v[188:189], s[50:51]\n"
      "v_cvt_pk_bf16_f32 v46, v188, v189\n"
      "v_lshlrev_b32 v190, 16, v47\n"
      "v_and_b32 v191, s54, v47\n"
      "v_pk_mul_f32 v[190:191], v[190:191], s[50:51]\n"
      "v_cvt_pk_bf16_f32 v47, v190, v191\n"
      "v_xor_b32 v195, 48, v235\n"
      "ds_write_b128 v195, v[44:47] offset:2304\n"
      "s_waitcnt vmcnt(3)\n"
      "v_lshlrev_b32 v176, 16, v48\n"
      "v_and_b32 v177, s54, v48\n"
      "v_pk_mul_f32 v[176:177], v[176:177], s[50:51]\n"
      "v_cvt_pk_bf16_f32 v48, v176, v177\n"
      "v_lshlrev_b32 v178, 16, v49\n"
      "v_and_b32 v179, s54, v49\n"
      "v_pk_mul_f32 v[178:179], v[178:179], s[50:51]\n"
      "v_cvt_pk_bf16_f32 v49, v178, v179\n"
      "v_lshlrev_b32 v180, 16, v50\n"
      "v_and_b32 v181, s54, v50\n"
      "v_pk_mul_f32 v[180:181], v[180:181], s[50:51]\n"
      "v_cvt_pk_bf16_f32 v50, v180, v181\n"
      "v_lshlrev_b32 v182, 16, v51\n"
      "v_and_b32 v183, s54, v51\n"
      "v_pk_mul_f32 v[182:183], v[182:183], s[50:51]\n"
      "v_cvt_pk_bf16_f32 v51, v182, v183\n"
      "v_xor_b32 v192, 0, v235\n"
      "ds_write_b128 v192, v[48:51] offset:4096\n"
      "s_waitcnt vmcnt(2)\n"
      "v_lshlrev_b32 v184, 16, v52\n"
      "v_and_b32 v185, s54, v52\n"
      "v_pk_mul_f32 v[184:185], v[184:185], s[50:51]\n"
      "v_cvt_pk_bf16_f32 v52, v184, v185\n"
      "v_lshlrev_b32 v186, 16, v53\n"
      "v_and_b32 v187, s54, v53\n"
      "v_pk_mul_f32 v[186:187], v[186:187], s[50:51]\n"
      "v_cvt_pk_bf16_f32 v53, v186, v187\n"
      "v_lshlrev_b32 v188, 16, v54\n"
      "v_and_b32 v189, s54, v54\n"
      "v_pk_mul_f32 v[188:189], v[188:189], s[50:51]\n"
      "v_cvt_pk_bf16_f32 v54, v188, v189\n"
      "v_lshlrev_b32 v190, 16, v55\n"
      "v_and_b32 v191, s54, v55\n"
      "v_pk_mul_f32 v[190:191], v[190:191], s[50:51]\n"
      "v_cvt_pk_bf16_f32 v55, v190, v191\n"
      "v_xor_b32 v193, 16, v235\n"
      "ds_write_b128 v193, v[52:55] offset:4352\n"
      "s_waitcnt vmcnt(1)\n"
      "v_lshlrev_b32 v176, 16, v56\n"
      "v_and_b32 v177, s54, v56\n"
      "v_pk_mul_f32 v[176:177], v[176:177], s[50:51]\n"
      "v_cvt_pk_bf16_f32 v56, v176, v177\n"
      "v_lshlrev_b32 v178, 16, v57\n"
      "v_and_b32 v179, s54, v57\n"
      "v_pk_mul_f32 v[178:179], v[178:179], s[50:51]\n"
      "v_cvt_pk_bf16_f32 v57, v178, v179\n"
      "v_lshlrev_b32 v180, 16, v58\n"
      "v_and_b32 v181, s54, v58\n"
      "v_pk_mul_f32 v[180:181], v[180:181], s[50:51]\n"
      "v_cvt_pk_bf16_f32 v58, v180, v181\n"
      "v_lshlrev_b32 v182, 16, v59\n"
      "v_and_b32 v183, s54, v59\n"
      "v_pk_mul_f32 v[182:183], v[182:183], s[50:51]\n"
      "v_cvt_pk_bf16_f32 v59, v182, v183\n"
      "v_xor_b32 v194, 32, v235\n"
      "ds_write_b128 v194, v[56:59] offset:6144\n"
      "s_waitcnt vmcnt(0)\n"
      "v_lshlrev_b32 v184, 16, v60\n"
      "v_and_b32 v185, s54, v60\n"
      "v_pk_mul_f32 v[184:185], v[184:185], s[50:51]\n"
      "v_cvt_pk_bf16_f32 v60, v184, v185\n"
      "v_lshlrev_b32 v186, 16, v61\n"
      "v_and_b32 v187, s54, v61\n"
      "v_pk_mul_f32 v[186:187], v[186:187], s[50:51]\n"
      "v_cvt_pk_bf16_f32 v61, v186, v187\n"
      "v_lshlrev_b32 v188, 16, v62\n"
      "v_and_b32 v189, s54, v62\n"
      "v_pk_mul_f32 v[188:189], v[188:189], s[50:51]\n"
      "v_cvt_pk_bf16_f32 v62, v188, v189\n"
      "v_lshlrev_b32 v190, 16, v63\n"
      "v_and_b32 v191, s54, v63\n"
      "v_pk_mul_f32 v[190:191], v[190:191], s[50:51]\n"
      "v_cvt_pk_bf16_f32 v63, v190, v191\n"
      "v_xor_b32 v195, 48, v235\n"
      "ds_write_b128 v195, v[60:63] offset:6400\n"
      "v_mov_b32 v176, 159744\n"
      "v_mov_b32 v177, 1\n"
      "s_waitcnt lgkmcnt(0)\n"
      "s_mov_b64 exec, 1\n"
      "ds_add_u32 v176, v177\n"
      "s_mov_b64 exec, -1\n"
      ".Lkspin_%=:\n"
      "ds_read_b32 v177, v176\n"
      "s_waitcnt lgkmcnt(0)\n"
      "v_readfirstlane_b32 s64, v177\n"
      "s_nop 3\n"
      "s_cmp_ge_u32 s64, 4\n"
      "s_cbranch_scc0 .Lkspin_%=\n"
      "s_branch .LspecialA_%=\n"
      ".LkB_%=:\n"
      "v_add_u32 v235, s74, v210\n"
      "v_mov_b32 v168, 0\n"
      "s_waitcnt vmcnt(32)\n"
      "v_and_b32 v160, s55, v32\n"
      "v_pk_max_u16 v168, v168, v160\n"
      "v_and_b32 v161, s55, v33\n"
      "v_pk_max_u16 v168, v168, v161\n"
      "v_and_b32 v162, s55, v34\n"
      "v_pk_max_u16 v168, v168, v162\n"
      "v_and_b32 v163, s55, v35\n"
      "v_pk_max_u16 v168, v168, v163\n"
      "v_and_b32 v160, s55, v36\n"
      "v_pk_max_u16 v168, v168, v160\n"
      "v_and_b32 v161, s55, v37\n"
      "v_pk_max_u16 v168, v168, v161\n"
      "v_and_b32 v162, s55, v38\n"
      "v_pk_max_u16 v168, v168, v162\n"
      "v_and_b32 v163, s55, v39\n"
      "v_pk_max_u16 v168, v168, v163\n"
      "v_and_b32 v160, s55, v40\n"
      "v_pk_max_u16 v168, v168, v160\n"
      "v_and_b32 v161, s55, v41\n"
      "v_pk_max_u16 v168, v168, v161\n"
      "v_and_b32 v162, s55, v42\n"
      "v_pk_max_u16 v168, v168, v162\n"
      "v_and_b32 v163, s55, v43\n"
      "v_pk_max_u16 v168, v168, v163\n"
      "v_and_b32 v160, s55, v44\n"
      "v_pk_max_u16 v168, v168, v160\n"
      "v_and_b32 v161, s55, v45\n"
      "v_pk_max_u16 v168, v168, v161\n"
      "v_and_b32 v162, s55, v46\n"
      "v_pk_max_u16 v168, v168, v162\n"
      "v_and_b32 v163, s55, v47\n"
      "v_pk_max_u16 v168, v168, v163\n"
      "v_perm_b32 v176, v34, v32, s52\n"
      "v_perm_b32 v177, v38, v36, s52\n"
      "v_perm_b32 v178, v42, v40, s52\n"
      "v_perm_b32 v179, v46, v44, s52\n"
      "ds_write_b128 v235, v[176:179] offset:0\n"
      "v_perm_b32 v180, v34, v32, s53\n"
      "v_perm_b32 v181, v38, v36, s53\n"
      "v_perm_b32 v182, v42, v40, s53\n"
      "v_perm_b32 v183, v46, v44, s53\n"
      "ds_write_b128 v235, v[180:183] offset:1024\n"
      "v_perm_b32 v184, v35, v33, s52\n"
      "v_perm_b32 v185, v39, v37, s52\n"
      "v_perm_b32 v186, v43, v41, s52\n"
      "v_perm_b32 v187, v47, v45, s52\n"
      "ds_write_b128 v235, v[184:187] offset:2048\n"
      "v_perm_b32 v188, v35, v33, s53\n"
      "v_perm_b32 v189, v39, v37, s53\n"
      "v_perm_b32 v190, v43, v41, s53\n"
      "v_perm_b32 v191, v47, v45, s53\n"
      "ds_write_b128 v235, v[188:191] offset:3072\n"
      "s_waitcnt vmcnt(24)\n"
      "v_and_b32 v160, s55, v48\n"
      "v_pk_max_u16 v168, v168, v160\n"
      "v_and_b32 v161, s55, v49\n"
      "v_pk_max_u16 v168, v168, v161\n"
      "v_and_b32 v162, s55, v50\n"
      "v_pk_max_u16 v168, v168, v162\n"
      "v_and_b32 v163, s55, v51\n"
      "v_pk_max_u16 v168, v168, v163\n"
      "v_and_b32 v160, s55, v52\n"
      "v_pk_max_u16 v168, v168, v160\n"
      "v_and_b32 v161, s55, v53\n"
      "v_pk_max_u16 v168, v168, v161\n"
      "v_and_b32 v162, s55, v54\n"
      "v_pk_max_u16 v168, v168, v162\n"
      "v_and_b32 v163, s55, v55\n"
      "v_pk_max_u16 v168, v168, v163\n"
      "v_and_b32 v160, s55, v56\n"
      "v_pk_max_u16 v168, v168, v160\n"
      "v_and_b32 v161, s55, v57\n"
      "v_pk_max_u16 v168, v168, v161\n"
      "v_and_b32 v162, s55, v58\n"
      "v_pk_max_u16 v168, v168, v162\n"
      "v_and_b32 v163, s55, v59\n"
      "v_pk_max_u16 v168, v168, v163\n"
      "v_and_b32 v160, s55, v60\n"
      "v_pk_max_u16 v168, v168, v160\n"
      "v_and_b32 v161, s55, v61\n"
      "v_pk_max_u16 v168, v168, v161\n"
      "v_and_b32 v162, s55, v62\n"
      "v_pk_max_u16 v168, v168, v162\n"
      "v_and_b32 v163, s55, v63\n"
      "v_pk_max_u16 v168, v168, v163\n"
      "v_perm_b32 v192, v50, v48, s52\n"
      "v_perm_b32 v193, v54, v52, s52\n"
      "v_perm_b32 v194, v58, v56, s52\n"
      "v_perm_b32 v195, v62, v60, s52\n"
      "ds_write_b128 v235, v[192:195] offset:4096\n"
      "v_perm_b32 v196, v50, v48, s53\n"
      "v_perm_b32 v197, v54, v52, s53\n"
      "v_perm_b32 v198, v58, v56, s53\n"
      "v_perm_b32 v199, v62, v60, s53\n"
      "ds_write_b128 v235, v[196:199] offset:5120\n"
      "v_perm_b32 v200, v51, v49, s52\n"
      "v_perm_b32 v201, v55, v53, s52\n"
      "v_perm_b32 v202, v59, v57, s52\n"
      "v_perm_b32 v203, v63, v61, s52\n"
      "ds_write_b128 v235, v[200:203] offset:6144\n"
      "v_perm_b32 v204, v51, v49, s53\n"
      "v_perm_b32 v205, v55, v53, s53\n"
      "v_perm_b32 v206, v59, v57, s53\n"
      "v_perm_b32 v207, v63, v61, s53\n"
      "ds_write_b128 v235, v[204:207] offset:7168\n"
      "s_waitcnt lgkmcnt(0)\n"
      "v_add_u32 v235, s71, v235\n"
      "s_add_u32 s64, s74, s71\n"
      "s_add_u32 s64, s64, 0x10000\n"
      "s_sub_u32 s64, s64, s63\n"
      "v_add_u32 v225, s64, v212\n"
      "v_add_u32 v226, s64, v213\n"
      "s_waitcnt vmcnt(16)\n"
      "ds_write_b128 v225, v[96:99] offset:0\n"
      "v_xor_b32 v218, 64, v225\n"
      "ds_write_b128 v218, v[100:103] offset:1024\n"
      "v_xor_b32 v219, 128, v225\n"
      "ds_write_b128 v219, v[104:107] offset:2048\n"
      "v_xor_b32 v220, 192, v225\n"
      "ds_write_b128 v220, v[108:111] offset:3072\n"
      "s_mov_b64 exec, s[58:59]\n"
      "ds_read_b128 v[64:67], v226\n"
      "v_xor_b32 v222, 32, v226\n"
      "ds_read_b128 v[68:71], v222\n"
      "v_xor_b32 v223, 64, v226\n"
      "ds_read_b128 v[72:75], v223\n"
      "v_xor_b32 v224, 96, v226\n"
      "ds_read_b128 v[76:79], v224\n"
      "v_xor_b32 v221, 128, v226\n"
      "ds_read_b128 v[80:83], v221\n"
      "v_xor_b32 v222, 160, v226\n"
      "ds_read_b128 v[84:87], v222\n"
      "v_xor_b32 v223, 192, v226\n"
      "ds_read_b128 v[88:91], v223\n"
      "v_xor_b32 v224, 224, v226\n"
      "ds_read_b128 v[92:95], v224\n"
      "s_mov_b64 exec, -1\n"
      "ds_write_b128 v225, v[112:115] offset:0\n"
      "v_xor_b32 v218, 64, v225\n"
      "ds_write_b128 v218, v[116:119] offset:1024\n"
      "v_xor_b32 v219, 128, v225\n"
      "ds_write_b128 v219, v[120:123] offset:2048\n"
      "v_xor_b32 v220, 192, v225\n"
      "ds_write_b128 v220, v[124:127] offset:3072\n"
      "s_mov_b64 exec, s[60:61]\n"
      "ds_read_b128 v[64:67], v226\n"
      "v_xor_b32 v222, 32, v226\n"
      "ds_read_b128 v[68:71], v222\n"
      "v_xor_b32 v223, 64, v226\n"
      "ds_read_b128 v[72:75], v223\n"
      "v_xor_b32 v224, 96, v226\n"
      "ds_read_b128 v[76:79], v224\n"
      "v_xor_b32 v221, 128, v226\n"
      "ds_read_b128 v[80:83], v221\n"
      "v_xor_b32 v222, 160, v226\n"
      "ds_read_b128 v[84:87], v222\n"
      "v_xor_b32 v223, 192, v226\n"
      "ds_read_b128 v[88:91], v223\n"
      "v_xor_b32 v224, 224, v226\n"
      "ds_read_b128 v[92:95], v224\n"
      "s_mov_b64 exec, -1\n"
      "s_waitcnt lgkmcnt(0)\n"
      "s_waitcnt vmcnt(8)\n"
      "v_and_b32 v160, s55, v128\n"
      "v_pk_max_u16 v168, v168, v160\n"
      "v_and_b32 v161, s55, v129\n"
      "v_pk_max_u16 v168, v168, v161\n"
      "v_and_b32 v162, s55, v130\n"
      "v_pk_max_u16 v168, v168, v162\n"
      "v_and_b32 v163, s55, v131\n"
      "v_pk_max_u16 v168, v168, v163\n"
      "v_and_b32 v160, s55, v132\n"
      "v_pk_max_u16 v168, v168, v160\n"
      "v_and_b32 v161, s55, v133\n"
      "v_pk_max_u16 v168, v168, v161\n"
      "v_and_b32 v162, s55, v134\n"
      "v_pk_max_u16 v168, v168, v162\n"
      "v_and_b32 v163, s55, v135\n"
      "v_pk_max_u16 v168, v168, v163\n"
      "v_and_b32 v160, s55, v136\n"
      "v_pk_max_u16 v168, v168, v160\n"
      "v_and_b32 v161, s55, v137\n"
      "v_pk_max_u16 v168, v168, v161\n"
      "v_and_b32 v162, s55, v138\n"
      "v_pk_max_u16 v168, v168, v162\n"
      "v_and_b32 v163, s55, v139\n"
      "v_pk_max_u16 v168, v168, v163\n"
      "v_and_b32 v160, s55, v140\n"
      "v_pk_max_u16 v168, v168, v160\n"
      "v_and_b32 v161, s55, v141\n"
      "v_pk_max_u16 v168, v168, v161\n"
      "v_and_b32 v162, s55, v142\n"
      "v_pk_max_u16 v168, v168, v162\n"
      "v_and_b32 v163, s55, v143\n"
      "v_pk_max_u16 v168, v168, v163\n"
      "v_perm_b32 v176, v130, v128, s52\n"
      "v_perm_b32 v177, v134, v132, s52\n"
      "v_perm_b32 v178, v138, v136, s52\n"
      "v_perm_b32 v179, v142, v140, s52\n"
      "ds_write_b128 v235, v[176:179] offset:0\n"
      "v_perm_b32 v180, v130, v128, s53\n"
      "v_perm_b32 v181, v134, v132, s53\n"
      "v_perm_b32 v182, v138, v136, s53\n"
      "v_perm_b32 v183, v142, v140, s53\n"
      "ds_write_b128 v235, v[180:183] offset:1024\n"
      "v_perm_b32 v184, v131, v129, s52\n"
      "v_perm_b32 v185, v135, v133, s52\n"
      "v_perm_b32 v186, v139, v137, s52\n"
      "v_perm_b32 v187, v143, v141, s52\n"
      "ds_write_b128 v235, v[184:187] offset:2048\n"
      "v_perm_b32 v188, v131, v129, s53\n"
      "v_perm_b32 v189, v135, v133, s53\n"
      "v_perm_b32 v190, v139, v137, s53\n"
      "v_perm_b32 v191, v143, v141, s53\n"
      "ds_write_b128 v235, v[188:191] offset:3072\n"
      "s_waitcnt vmcnt(0)\n"
      "v_and_b32 v160, s55, v144\n"
      "v_pk_max_u16 v168, v168, v160\n"
      "v_and_b32 v161, s55, v145\n"
      "v_pk_max_u16 v168, v168, v161\n"
      "v_and_b32 v162, s55, v146\n"
      "v_pk_max_u16 v168, v168, v162\n"
      "v_and_b32 v163, s55, v147\n"
      "v_pk_max_u16 v168, v168, v163\n"
      "v_and_b32 v160, s55, v148\n"
      "v_pk_max_u16 v168, v168, v160\n"
      "v_and_b32 v161, s55, v149\n"
      "v_pk_max_u16 v168, v168, v161\n"
      "v_and_b32 v162, s55, v150\n"
      "v_pk_max_u16 v168, v168, v162\n"
      "v_and_b32 v163, s55, v151\n"
      "v_pk_max_u16 v168, v168, v163\n"
      "v_and_b32 v160, s55, v152\n"
      "v_pk_max_u16 v168, v168, v160\n"
      "v_and_b32 v161, s55, v153\n"
      "v_pk_max_u16 v168, v168, v161\n"
      "v_and_b32 v162, s55, v154\n"
      "v_pk_max_u16 v168, v168, v162\n"
      "v_and_b32 v163, s55, v155\n"
      "v_pk_max_u16 v168, v168, v163\n"
      "v_and_b32 v160, s55, v156\n"
      "v_pk_max_u16 v168, v168, v160\n"
      "v_and_b32 v161, s55, v157\n"
      "v_pk_max_u16 v168, v168, v161\n"
      "v_and_b32 v162, s55, v158\n"
      "v_pk_max_u16 v168, v168, v162\n"
      "v_and_b32 v163, s55, v159\n"
      "v_pk_max_u16 v168, v168, v163\n"
      "v_perm_b32 v192, v146, v144, s52\n"
      "v_perm_b32 v193, v150, v148, s52\n"
      "v_perm_b32 v194, v154, v152, s52\n"
      "v_perm_b32 v195, v158, v156, s52\n"
      "ds_write_b128 v235, v[192:195] offset:4096\n"
      "v_perm_b32 v196, v146, v144, s53\n"
      "v_perm_b32 v197, v150, v148, s53\n"
      "v_perm_b32 v198, v154, v152, s53\n"
      "v_perm_b32 v199, v158, v156, s53\n"
      "ds_write_b128 v235, v[196:199] offset:5120\n"
      "v_perm_b32 v200, v147, v145, s52\n"
      "v_perm_b32 v201, v151, v149, s52\n"
      "v_perm_b32 v202, v155, v153, s52\n"
      "v_perm_b32 v203, v159, v157, s52\n"
      "ds_write_b128 v235, v[200:203] offset:6144\n"
      "v_perm_b32 v204, v147, v145, s53\n"
      "v_perm_b32 v205, v151, v149, s53\n"
      "v_perm_b32 v206, v155, v153, s53\n"
      "v_perm_b32 v207, v159, v157, s53\n"
      "ds_write_b128 v235, v[204:207] offset:7168\n"
      "s_waitcnt lgkmcnt(0)\n"
      "v_lshrrev_b32 v160, 16, v168\n"
      "v_and_b32 v161, 0xffff, v168\n"
      "v_max_u32 v160, v160, v161\n"
      "v_cmp_le_u32 vcc, 0x5f80, v160\n"
      "v_cmp_le_u32 s[66:67], s72, v160\n"
      "s_nop 4\n"
      "s_cmp_lg_u64 vcc, 0\n"
      "s_cselect_b32 s65, 1, 0\n"
      "s_cmp_lg_u64 s[66:67], 0\n"
      "s_cselect_b32 s64, 2, 0\n"
      "s_or_b32 s65, s65, s64\n"
      "s_lshl_b32 s64, s49, 2\n"
      "s_add_u32 s64, s64, 159744\n"
      "v_mov_b32 v160, s65\n"
      "v_mov_b32 v161, s64\n"
      "ds_write_b32 v161, v160\n"
      "s_waitcnt lgkmcnt(0)\n"
      "s_barrier\n"
      "v_mov_b32 v217, 159760\n"
      "ds_read_b128 v[176:179], v217\n"
      "s_waitcnt lgkmcnt(0)\n"
      "v_or_b32 v176, v176, v177\n"
      "v_or3_b32 v176, v176, v178, v179\n"
      "v_cmp_ne_u32 vcc, 2, v176\n"
      "s_nop 4\n"
      "s_cmp_lg_u64 vcc, 0\n"
      "s_cselect_b32 s62, 1, 0\n"
      "s_barrier\n"
      "s_waitcnt vmcnt(0)\n"
      "v_xor_b32 v236, 64, v212\n"
      "v_xor_b32 v237, 128, v212\n"
      "v_xor_b32 v238, 192, v212\n"
      "v_xor_b32 v239, 32, v213\n"
      "v_xor_b32 v240, 64, v213\n"
      "v_xor_b32 v241, 96, v213\n"
      "v_xor_b32 v242, 128, v213\n"
      "v_xor_b32 v243, 160, v213\n"
      "v_xor_b32 v244, 192, v213\n"
      "v_xor_b32 v245, 224, v213\n"
      "s_waitcnt lgkmcnt(0)\n"
      "s_add_u32 s40, s40, 0x10000\n"
      "s_addc_u32 s41, s41, 0\n"
      "global_load_dwordx4 v[96:99], v211, s[40:41] offset:-4096 nt\n"
      "global_load_dwordx4 v[100:103], v211, s[40:41] offset:-3072 nt\n"
      "global_load_dwordx4 v[104:107], v211, s[40:41] offset:-2048 nt\n"
      "global_load_dwordx4 v[108:111], v211, s[40:41] offset:-1024 nt\n"
      "global_load_dwordx4 v[112:115], v211, s[40:41] offset:0 nt\n"
      "global_load_dwordx4 v[116:119], v211, s[40:41] offset:1024 nt\n"
      "global_load_dwordx4 v[120:123], v211, s[40:41] offset:2048 nt\n"
      "global_load_dwordx4 v[124:127], v211, s[40:41] offset:3072 nt\n"
      "s_branch .Lblk_%=\n"
      ".LspecialA_%=:\n"
      "s_add_u32 s40, s40, 0x10000\n"
      "s_addc_u32 s41, s41, 0\n"
      "s_setprio 1\n"
      "ds_read_b128 v[176:179], v208 offset:0\n"
      "ds_read_b128 v[180:183], v209 offset:0\n"
      "ds_read_b128 v[184:187], v208 offset:512\n"
      "ds_read_b128 v[188:191], v209 offset:512\n"
      "ds_read_b128 v[192:195], v208 offset:1024\n"
      "ds_read_b128 v[196:199], v209 offset:1024\n"
      "ds_read_b128 v[200:203], v208 offset:1536\n"
      "ds_read_b128 v[204:207], v209 offset:1536\n"
      "s_waitcnt lgkmcnt(4)\n"
      "v_mfma_f32_32x32x16_bf16 v[128:143], v[176:179], v[64:67], 0\n"
      "ds_read_b128 v[176:179], v208 offset:8192\n"
      "v_mfma_f32_32x32x16_bf16 v[128:143], v[180:183], v[68:71], v[128:143]\n"
      "ds_read_b128 v[180:183], v209 offset:8192\n"
      "v_mfma_f32_32x32x16_bf16 v[128:143], v[184:187], v[72:75], v[128:143]\n"
      "ds_read_b128 v[184:187], v208 offset:8704\n"
      "v_mfma_f32_32x32x16_bf16 v[128:143], v[188:191], v[76:79], v[128:143]\n"
      "ds_read_b128 v[188:191], v209 offset:8704\n"
      "s_waitcnt lgkmcnt(4)\n"
      "v_mfma_f32_32x32x16_bf16 v[128:143], v[192:195], v[80:83], v[128:143]\n"
      "ds_read_b128 v[192:195], v208 offset:9216\n"
      "v_mfma_f32_32x32x16_bf16 v[128:143], v[196:199], v[84:87], v[128:143]\n"
      "ds_read_b128 v[196:199], v209 offset:9216\n"
      "v_mfma_f32_32x32x16_bf16 v[128:143], v[200:203], v[88:91], v[128:143]\n"
      "ds_read_b128 v[200:203], v208 offset:9728\n"
      "v_mfma_f32_32x32x16_bf16 v[128:143], v[204:207], v[92:95], v[128:143]\n"
      "ds_read_b128 v[204:207], v209 offset:9728\n"
      "s_waitcnt lgkmcnt(4)\n"
      "v_mfma_f32_32x32x16_bf16 v[144:159], v[176:179], v[64:67], 0\n"
      "ds_read_b128 v[176:179], v208 offset:16384\n"
      "v_mfma_f32_32x32x16_bf16 v[144:159], v[180:183], v[68:71], v[144:159]\n"
      "ds_read_b128 v[180:183], v209 offset:16384\n"
      "s_nop 5\n"
      "v_exp_f32 v128, v128\n"
      "v_exp_f32 v129, v129\n"
      "v_exp_f32 v130, v130\n"
      "v_mfma_f32_32x32x16_bf16 v[144:159], v[184:187], v[72:75], v[144:159]\n"
      "ds_read_b128 v[184:187], v208 offset:16896\n"
      "v_exp_f32 v131, v131\n"
      "v_exp_f32 v132, v132\n"
      "v_exp_f32 v133, v133\n"
      "v_mov_b32 v215, v128\n"
      "v_mov_b32 v216, v129\n"
      "v_add_f32 v215, v130, v215\n"
      "v_cvt_pk_bf16_f32 v160, v128, v129\n"
      "v_mfma_f32_32x32x16_bf16 v[144:159], v[188:191], v[76:79], v[144:159]\n"
      "ds_read_b128 v[188:191], v209 offset:16896\n"
      "v_exp_f32 v134, v134\n"
      "v_exp_f32 v135, v135\n"
      "v_add_f32 v216, v131, v216\n"
      "v_add_f32 v215, v132, v215\n"
      "v_add_f32 v216, v133, v216\n"
      "v_cvt_pk_bf16_f32 v161, v130, v131\n"
      "v_cvt_pk_bf16_f32 v162, v132, v133\n"
      "s_waitcnt lgkmcnt(4)\n"
      "v_mfma_f32_32x32x16_bf16 v[144:159], v[192:195], v[80:83], v[144:159]\n"
      "ds_read_b128 v[192:195], v208 offset:17408\n"
      "v_exp_f32 v136, v136\n"
      "v_exp_f32 v137, v137\n"
      "v_exp_f32 v138, v138\n"
      "v_add_f32 v215, v134, v215\n"
      "v_add_f32 v216, v135, v216\n"
      "v_cvt_pk_bf16_f32 v163, v134, v135\n"
      "v_mfma_f32_32x32x16_bf16 v[144:159], v[196:199], v[84:87], v[144:159]\n"
      "ds_read_b128 v[196:199], v209 offset:17408\n"
      "v_exp_f32 v139, v139\n"
      "v_exp_f32 v140, v140\n"
      "v_exp_f32 v141, v141\n"
      "v_add_f32 v215, v136, v215\n"
      "v_add_f32 v216, v137, v216\n"
      "v_add_f32 v215, v138, v215\n"
      "v_cvt_pk_bf16_f32 v164, v136, v137\n"
      "v_mfma_f32_32x32x16_bf16 v[144:159], v[200:203], v[88:91], v[144:159]\n"
      "ds_read_b128 v[200:203], v208 offset:17920\n"
      "v_exp_f32 v142, v142\n"
      "v_exp_f32 v143, v143\n"
      "v_add_f32 v216, v139, v216\n"
      "v_add_f32 v215, v140, v215\n"
      "v_add_f32 v216, v141, v216\n"
      "v_cvt_pk_bf16_f32 v165, v138, v139\n"
      "v_cvt_pk_bf16_f32 v166, v140, v141\n"
      "v_mfma_f32_32x32x16_bf16 v[144:159], v[204:207], v[92:95], v[144:159]\n"
      "ds_read_b128 v[204:207], v209 offset:17920\n"
      "v_add_f32 v215, v142, v215\n"
      "v_add_f32 v216, v143, v216\n"
      "v_cvt_pk_bf16_f32 v167, v142, v143\n"
      "s_waitcnt lgkmcnt(4)\n"
      "v_mfma_f32_32x32x16_bf16 v[128:143], v[176:179], v[64:67], 0\n"
      "ds_read_b128 v[176:179], v208 offset:24576\n"
      "v_mfma_f32_32x32x16_bf16 v[128:143], v[180:183], v[68:71], v[128:143]\n"
      "ds_read_b128 v[180:183], v209 offset:24576\n"
      "s_nop 2\n"
      "v_exp_f32 v144, v144\n"
      "v_exp_f32 v145, v145\n"
      "v_exp_f32 v146, v146\n"
      "v_mfma_f32_32x32x16_bf16 v[128:143], v[184:187], v[72:75], v[128:143]\n"
      "ds_read_b128 v[184:187], v208 offset:25088\n"
      "v_exp_f32 v147, v147\n"
      "v_exp_f32 v148, v148\n"
      "v_exp_f32 v149, v149\n"
      "v_add_f32 v215, v144, v215\n"
      "v_add_f32 v216, v145, v216\n"
      "v_add_f32 v215, v146, v215\n"
      "v_cvt_pk_bf16_f32 v168, v144, v145\n"
      "v_mfma_f32_32x32x16_bf16 v[128:143], v[188:191], v[76:79], v[128:143]\n"
      "ds_read_b128 v[188:191], v209 offset:25088\n"
      "v_exp_f32 v150, v150\n"
      "v_exp_f32 v151, v151\n"
      "v_add_f32 v216, v147, v216\n"
      "v_add_f32 v215, v148, v215\n"
      "v_add_f32 v216, v149, v216\n"
      "v_cvt_pk_bf16_f32 v169, v146, v147\n"
      "v_cvt_pk_bf16_f32 v170, v148, v149\n"
      "s_waitcnt lgkmcnt(4)\n"
      "v_mfma_f32_32x32x16_bf16 v[128:143], v[192:195], v[80:83], v[128:143]\n"
      "ds_read_b128 v[192:195], v208 offset:25600\n"
      "v_exp_f32 v152, v152\n"
      "v_exp_f32 v153, v153\n"
      "v_exp_f32 v154, v154\n"
      "v_add_f32 v215, v150, v215\n"
      "v_add_f32 v216, v151, v216\n"
      "v_cvt_pk_bf16_f32 v171, v150, v151\n"
      "v_mfma_f32_32x32x16_bf16 v[128:143], v[196:199], v[84:87], v[128:143]\n"
      "ds_read_b128 v[196:199], v209 offset:25600\n"
      "v_exp_f32 v155, v155\n"
      "v_exp_f32 v156, v156\n"
      "v_exp_f32 v157, v157\n"
      "v_add_f32 v215, v152, v215\n"
      "v_add_f32 v216, v153, v216\n"
      "v_add_f32 v215, v154, v215\n"
      "v_cvt_pk_bf16_f32 v172, v152, v153\n"
      "v_mfma_f32_32x32x16_bf16 v[128:143], v[200:203], v[88:91], v[128:143]\n"
      "ds_read_b128 v[200:203], v208 offset:26112\n"
      "v_exp_f32 v158, v158\n"
      "v_exp_f32 v159, v159\n"
      "v_add_f32 v216, v155, v216\n"
      "v_add_f32 v215, v156, v215\n"
      "v_add_f32 v216, v157, v216\n"
      "v_cvt_pk_bf16_f32 v173, v154, v155\n"
      "v_cvt_pk_bf16_f32 v174, v156, v157\n"
      "v_mfma_f32_32x32x16_bf16 v[128:143], v[204:207], v[92:95], v[128:143]\n"
      "ds_read_b128 v[204:207], v209 offset:26112\n"
      "v_add_f32 v215, v158, v215\n"
      "v_add_f32 v216, v159, v216\n"
      "v_cvt_pk_bf16_f32 v175, v158, v159\n"
      "s_waitcnt lgkmcnt(4)\n"
      "v_mfma_f32_32x32x16_bf16 v[144:159], v[176:179], v[64:67], 0\n"
      "ds_read_b128 v[176:179], v208 offset:32768\n"
      "v_mfma_f32_32x32x16_bf16 v[144:159], v[180:183], v[68:71], v[144:159]\n"
      "ds_read_b128 v[180:183], v209 offset:32768\n"
      "s_nop 2\n"
      "v_exp_f32 v128, v128\n"
      "v_exp_f32 v129, v129\n"
      "v_exp_f32 v130, v130\n"
      "v_mfma_f32_32x32x16_bf16 v[144:159], v[184:187], v[72:75], v[144:159]\n"
      "ds_read_b128 v[184:187], v208 offset:33280\n"
      "v_exp_f32 v131, v131\n"
      "v_exp_f32 v132, v132\n"
      "v_exp_f32 v133, v133\n"
      "v_add_f32 v215, v128, v215\n"
      "v_add_f32 v216, v129, v216\n"
      "v_add_f32 v215, v130, v215\n"
      "v_cvt_pk_bf16_f32 v236, v128, v129\n"
      "v_mfma_f32_32x32x16_bf16 v[144:159], v[188:191], v[76:79], v[144:159]\n"
      "ds_read_b128 v[188:191], v209 offset:33280\n"
      "v_exp_f32 v134, v134\n"
      "v_exp_f32 v135, v135\n"
      "v_add_f32 v216, v131, v216\n"
      "v_add_f32 v215, v132, v215\n"
      "v_add_f32 v216, v133, v216\n"
      "v_cvt_pk_bf16_f32 v237, v130, v131\n"
      "v_cvt_pk_bf16_f32 v238, v132, v133\n"
      "s_waitcnt lgkmcnt(4)\n"
      "v_mfma_f32_32x32x16_bf16 v[144:159], v[192:195], v[80:83], v[144:159]\n"
      "ds_read_b128 v[192:195], v208 offset:33792\n"
      "v_exp_f32 v136, v136\n"
      "v_exp_f32 v137, v137\n"
      "v_exp_f32 v138, v138\n"
      "v_add_f32 v215, v134, v215\n"
      "v_add_f32 v216, v135, v216\n"
      "v_cvt_pk_bf16_f32 v239, v134, v135\n"
      "v_mfma_f32_32x32x16_bf16 v[144:159], v[196:199], v[84:87], v[144:159]\n"
      "ds_read_b128 v[196:199], v209 offset:33792\n"
      "v_exp_f32 v139, v139\n"
      "v_exp_f32 v140, v140\n"
      "v_exp_f32 v141, v141\n"
      "v_add_f32 v215, v136, v215\n"
      "v_add_f32 v216, v137, v216\n"
      "v_add_f32 v215, v138, v215\n"
      "v_cvt_pk_bf16_f32 v240, v136, v137\n"
      "v_mfma_f32_32x32x16_bf16 v[144:159], v[200:203], v[88:91], v[144:159]\n"
      "ds_read_b128 v[200:203], v208 offset:34304\n"
      "v_exp_f32 v142, v142\n"
      "v_exp_f32 v143, v143\n"
      "v_add_f32 v216, v139, v216\n"
      "v_add_f32 v215, v140, v215\n"
      "v_add_f32 v216, v141, v216\n"
      "v_cvt_pk_bf16_f32 v241, v138, v139\n"
      "v_cvt_pk_bf16_f32 v242, v140, v141\n"
      "v_mfma_f32_32x32x16_bf16 v[144:159], v[204:207], v[92:95], v[144:159]\n"
      "ds_read_b128 v[204:207], v209 offset:34304\n"
      "v_add_f32 v215, v142, v215\n"
      "v_add_f32 v216, v143, v216\n"
      "v_cvt_pk_bf16_f32 v243, v142, v143\n"
      "s_waitcnt lgkmcnt(4)\n"
      "v_mfma_f32_32x32x16_bf16 v[128:143], v[176:179], v[64:67], 0\n"
      "ds_read_b128 v[176:179], v208 offset:40960\n"
      "v_mfma_f32_32x32x16_bf16 v[128:143], v[180:183], v[68:71], v[128:143]\n"
      "ds_read_b128 v[180:183], v209 offset:40960\n"
      "s_nop 2\n"
      "v_exp_f32 v144, v144\n"
      "v_exp_f32 v145, v145\n"
      "v_exp_f32 v146, v146\n"
      "v_mfma_f32_32x32x16_bf16 v[128:143], v[184:187], v[72:75], v[128:143]\n"
      "ds_read_b128 v[184:187], v208 offset:41472\n"
      "v_exp_f32 v147, v147\n"
      "v_exp_f32 v148, v148\n"
      "v_exp_f32 v149, v149\n"
      "v_add_f32 v215, v144, v215\n"
      "v_add_f32 v216, v145, v216\n"
      "v_add_f32 v215, v146, v215\n"
      "v_cvt_pk_bf16_f32 v244, v144, v145\n"
      "v_mfma_f32_32x32x16_bf16 v[128:143], v[188:191], v[76:79], v[128:143]\n"
      "ds_read_b128 v[188:191], v209 offset:41472\n"
      "v_exp_f32 v150, v150\n"
      "v_exp_f32 v151, v151\n"
      "v_add_f32 v216, v147, v216\n"
      "v_add_f32 v215, v148, v215\n"
      "v_add_f32 v216, v149, v216\n"
      "v_cvt_pk_bf16_f32 v245, v146, v147\n"
      "v_cvt_pk_bf16_f32 v246, v148, v149\n"
      "s_waitcnt lgkmcnt(4)\n"
      "v_mfma_f32_32x32x16_bf16 v[128:143], v[192:195], v[80:83], v[128:143]\n"
      "ds_read_b128 v[192:195], v208 offset:41984\n"
      "v_exp_f32 v152, v152\n"
      "v_exp_f32 v153, v153\n"
      "v_exp_f32 v154, v154\n"
      "v_add_f32 v215, v150, v215\n"
      "v_add_f32 v216, v151, v216\n"
      "v_cvt_pk_bf16_f32 v247, v150, v151\n"
      "v_mfma_f32_32x32x16_bf16 v[128:143], v[196:199], v[84:87], v[128:143]\n"
      "ds_read_b128 v[196:199], v209 offset:41984\n"
      "v_exp_f32 v155, v155\n"
      "v_exp_f32 v156, v156\n"
      "v_exp_f32 v157, v157\n"
      "v_add_f32 v215, v152, v215\n"
      "v_add_f32 v216, v153, v216\n"
      "v_add_f32 v215, v154, v215\n"
      "v_cvt_pk_bf16_f32 v248, v152, v153\n"
      "v_mfma_f32_32x32x16_bf16 v[128:143], v[200:203], v[88:91], v[128:143]\n"
      "ds_read_b128 v[200:203], v208 offset:42496\n"
      "v_exp_f32 v158, v158\n"
      "v_exp_f32 v159, v159\n"
      "v_add_f32 v216, v155, v216\n"
      "v_add_f32 v215, v156, v215\n"
      "v_add_f32 v216, v157, v216\n"
      "v_cvt_pk_bf16_f32 v249, v154, v155\n"
      "v_cvt_pk_bf16_f32 v250, v156, v157\n"
      "v_mfma_f32_32x32x16_bf16 v[128:143], v[204:207], v[92:95], v[128:143]\n"
      "ds_read_b128 v[204:207], v209 offset:42496\n"
      "v_add_f32 v215, v158, v215\n"
      "v_add_f32 v216, v159, v216\n"
      "v_cvt_pk_bf16_f32 v251, v158, v159\n"
      "s_waitcnt lgkmcnt(4)\n"
      "v_mfma_f32_32x32x16_bf16 v[144:159], v[176:179], v[64:67], 0\n"
      "ds_read_b128 v[176:179], v208 offset:49152\n"
      "v_mfma_f32_32x32x16_bf16 v[144:159], v[180:183], v[68:71], v[144:159]\n"
      "ds_read_b128 v[180:183], v209 offset:49152\n"
      "s_nop 2\n"
      "v_exp_f32 v128, v128\n"
      "v_exp_f32 v129, v129\n"
      "v_exp_f32 v130, v130\n"
      "v_mfma_f32_32x32x16_bf16 v[144:159], v[184:187], v[72:75], v[144:159]\n"
      "ds_read_b128 v[184:187], v208 offset:49664\n"
      "v_exp_f32 v131, v131\n"
      "v_exp_f32 v132, v132\n"
      "v_exp_f32 v133, v133\n"
      "v_add_f32 v215, v128, v215\n"
      "v_add_f32 v216, v129, v216\n"
      "v_add_f32 v215, v130, v215\n"
      "v_cvt_pk_bf16_f32 v218, v128, v129\n"
      "v_mfma_f32_32x32x16_bf16 v[144:159], v[188:191], v[76:79], v[144:159]\n"
      "ds_read_b128 v[188:191], v209 offset:49664\n"
      "v_exp_f32 v134, v134\n"
      "v_exp_f32 v135, v135\n"
      "v_add_f32 v216, v131, v216\n"
      "v_add_f32 v215, v132, v215\n"
      "v_add_f32 v216, v133, v216\n"
      "v_cvt_pk_bf16_f32 v219, v130, v131\n"
      "v_cvt_pk_bf16_f32 v220, v132, v133\n"
      "s_waitcnt lgkmcnt(4)\n"
      "v_mfma_f32_32x32x16_bf16 v[144:159], v[192:195], v[80:83], v[144:159]\n"
      "ds_read_b128 v[192:195], v208 offset:50176\n"
      "v_exp_f32 v136, v136\n"
      "v_exp_f32 v137, v137\n"
      "v_exp_f32 v138, v138\n"
      "v_add_f32 v215, v134, v215\n"
      "v_add_f32 v216, v135, v216\n"
      "v_cvt_pk_bf16_f32 v221, v134, v135\n"
      "v_mfma_f32_32x32x16_bf16 v[144:159], v[196:199], v[84:87], v[144:159]\n"
      "ds_read_b128 v[196:199], v209 offset:50176\n"
      "v_exp_f32 v139, v139\n"
      "v_exp_f32 v140, v140\n"
      "v_exp_f32 v141, v141\n"
      "v_add_f32 v215, v136, v215\n"
      "v_add_f32 v216, v137, v216\n"
      "v_add_f32 v215, v138, v215\n"
      "v_cvt_pk_bf16_f32 v222, v136, v137\n"
      "v_mfma_f32_32x32x16_bf16 v[144:159], v[200:203], v[88:91], v[144:159]\n"
      "ds_read_b128 v[200:203], v208 offset:50688\n"
      "v_exp_f32 v142, v142\n"
      "v_exp_f32 v143, v143\n"
      "v_add_f32 v216, v139, v216\n"
      "v_add_f32 v215, v140, v215\n"
      "v_add_f32 v216, v141, v216\n"
      "v_cvt_pk_bf16_f32 v223, v138, v139\n"
      "v_cvt_pk_bf16_f32 v224, v140, v141\n"
      "v_mfma_f32_32x32x16_bf16 v[144:159], v[204:207], v[92:95], v[144:159]\n"
      "ds_read_b128 v[204:207], v209 offset:50688\n"
      "v_add_f32 v215, v142, v215\n"
      "v_add_f32 v216, v143, v216\n"
      "v_cvt_pk_bf16_f32 v225, v142, v143\n"
      "s_waitcnt lgkmcnt(4)\n"
      "v_mfma_f32_32x32x16_bf16 v[128:143], v[176:179], v[64:67], 0\n"
      "ds_read_b128 v[176:179], v208 offset:57344\n"
      "v_mfma_f32_32x32x16_bf16 v[128:143], v[180:183], v[68:71], v[128:143]\n"
      "ds_read_b128 v[180:183], v209 offset:57344\n"
      "s_nop 2\n"
      "v_exp_f32 v144, v144\n"
      "v_exp_f32 v145, v145\n"
      "v_exp_f32 v146, v146\n"
      "v_mfma_f32_32x32x16_bf16 v[128:143], v[184:187], v[72:75], v[128:143]\n"
      "ds_read_b128 v[184:187], v208 offset:57856\n"
      "v_exp_f32 v147, v147\n"
      "v_exp_f32 v148, v148\n"
      "v_exp_f32 v149, v149\n"
      "v_add_f32 v215, v144, v215\n"
      "v_add_f32 v216, v145, v216\n"
      "v_add_f32 v215, v146, v215\n"
      "v_cvt_pk_bf16_f32 v230, v144, v145\n"
      "v_mfma_f32_32x32x16_bf16 v[128:143], v[188:191], v[76:79], v[128:143]\n"
      "ds_read_b128 v[188:191], v209 offset:57856\n"
      "v_exp_f32 v150, v150\n"
      "v_exp_f32 v151, v151\n"
      "v_add_f32 v216, v147, v216\n"
      "v_add_f32 v215, v148, v215\n"
      "v_add_f32 v216, v149, v216\n"
      "v_cvt_pk_bf16_f32 v231, v146, v147\n"
      "v_cvt_pk_bf16_f32 v232, v148, v149\n"
      "s_waitcnt lgkmcnt(4)\n"
      "v_mfma_f32_32x32x16_bf16 v[128:143], v[192:195], v[80:83], v[128:143]\n"
      "ds_read_b128 v[192:195], v208 offset:58368\n"
      "v_exp_f32 v152, v152\n"
      "v_exp_f32 v153, v153\n"
      "v_exp_f32 v154, v154\n"
      "v_add_f32 v215, v150, v215\n"
      "v_add_f32 v216, v151, v216\n"
      "v_cvt_pk_bf16_f32 v233, v150, v151\n"
      "v_mfma_f32_32x32x16_bf16 v[128:143], v[196:199], v[84:87], v[128:143]\n"
      "ds_read_b128 v[196:199], v209 offset:58368\n"
      "v_exp_f32 v155, v155\n"
      "v_exp_f32 v156, v156\n"
      "v_exp_f32 v157, v157\n"
      "v_add_f32 v215, v152, v215\n"
      "v_add_f32 v216, v153, v216\n"
      "v_add_f32 v215, v154, v215\n"
      "v_cvt_pk_bf16_f32 v252, v152, v153\n"
      "v_mfma_f32_32x32x16_bf16 v[128:143], v[200:203], v[88:91], v[128:143]\n"
      "ds_read_b128 v[200:203], v208 offset:58880\n"
      "v_exp_f32 v158, v158\n"
      "v_exp_f32 v159, v159\n"
      "v_add_f32 v216, v155, v216\n"
      "v_add_f32 v215, v156, v215\n"
      "v_add_f32 v216, v157, v216\n"
      "v_cvt_pk_bf16_f32 v253, v154, v155\n"
      "v_cvt_pk_bf16_f32 v254, v156, v157\n"
      "v_mfma_f32_32x32x16_bf16 v[128:143], v[204:207], v[92:95], v[128:143]\n"
      "ds_read_b128 v[204:207], v209 offset:58880\n"
      "v_add_f32 v215, v158, v215\n"
      "v_add_f32 v216, v159, v216\n"
      "v_cvt_pk_bf16_f32 v255, v158, v159\n"
      "s_waitcnt lgkmcnt(4)\n"
      "v_mfma_f32_32x32x16_bf16 v[144:159], v[176:179], v[64:67], 0\n"
      "v_mfma_f32_32x32x16_bf16 v[144:159], v[180:183], v[68:71], v[144:159]\n"
      "s_nop 4\n"
      "v_exp_f32 v128, v128\n"
      "v_exp_f32 v129, v129\n"
      "v_exp_f32 v130, v130\n"
      "v_mfma_f32_32x32x16_bf16 v[144:159], v[184:187], v[72:75], v[144:159]\n"
      "v_exp_f32 v131, v131\n"
      "v_exp_f32 v132, v132\n"
      "v_exp_f32 v133, v133\n"
      "v_add_f32 v215, v128, v215\n"
      "v_add_f32 v216, v129, v216\n"
      "v_add_f32 v215, v130, v215\n"
      "v_cvt_pk_bf16_f32 v128, v128, v129\n"
      "v_mfma_f32_32x32x16_bf16 v[144:159], v[188:191], v[76:79], v[144:159]\n"
      "v_exp_f32 v134, v134\n"
      "v_exp_f32 v135, v135\n"
      "v_add_f32 v216, v131, v216\n"
      "v_add_f32 v215, v132, v215\n"
      "v_add_f32 v216, v133, v216\n"
      "v_cvt_pk_bf16_f32 v129, v130, v131\n"
      "v_cvt_pk_bf16_f32 v130, v132, v133\n"
      "s_waitcnt lgkmcnt(0)\n"
      "v_mfma_f32_32x32x16_bf16 v[144:159], v[192:195], v[80:83], v[144:159]\n"
      "v_exp_f32 v136, v136\n"
      "v_exp_f32 v137, v137\n"
      "v_exp_f32 v138, v138\n"
      "v_add_f32 v215, v134, v215\n"
      "v_add_f32 v216, v135, v216\n"
      "v_cvt_pk_bf16_f32 v131, v134, v135\n"
      "v_mfma_f32_32x32x16_bf16 v[144:159], v[196:199], v[84:87], v[144:159]\n"
      "v_exp_f32 v139, v139\n"
      "v_exp_f32 v140, v140\n"
      "v_exp_f32 v141, v141\n"
      "v_add_f32 v215, v136, v215\n"
      "v_add_f32 v216, v137, v216\n"
      "v_add_f32 v215, v138, v215\n"
      "v_cvt_pk_bf16_f32 v132, v136, v137\n"
      "v_mfma_f32_32x32x16_bf16 v[144:159], v[200:203], v[88:91], v[144:159]\n"
      "v_exp_f32 v142, v142\n"
      "v_exp_f32 v143, v143\n"
      "v_add_f32 v216, v139, v216\n"
      "v_add_f32 v215, v140, v215\n"
      "v_add_f32 v216, v141, v216\n"
      "v_cvt_pk_bf16_f32 v133, v138, v139\n"
      "v_cvt_pk_bf16_f32 v134, v140, v141\n"
      "v_mfma_f32_32x32x16_bf16 v[144:159], v[204:207], v[92:95], v[144:159]\n"
      "v_add_f32 v215, v142, v215\n"
      "v_add_f32 v216, v143, v216\n"
      "v_cvt_pk_bf16_f32 v135, v142, v143\n"
      "s_nop 7\n"
      "s_nop 0\n"
      "v_exp_f32 v144, v144\n"
      "v_exp_f32 v145, v145\n"
      "v_exp_f32 v146, v146\n"
      "v_exp_f32 v147, v147\n"
      "v_exp_f32 v148, v148\n"
      "v_exp_f32 v149, v149\n"
      "v_add_f32 v215, v144, v215\n"
      "v_add_f32 v216, v145, v216\n"
      "v_add_f32 v215, v146, v215\n"
      "v_cvt_pk_bf16_f32 v144, v144, v145\n"
      "v_exp_f32 v150, v150\n"
      "v_exp_f32 v151, v151\n"
      "v_add_f32 v216, v147, v216\n"
      "v_add_f32 v215, v148, v215\n"
      "v_add_f32 v216, v149, v216\n"
      "v_cvt_pk_bf16_f32 v145, v146, v147\n"
      "v_cvt_pk_bf16_f32 v146, v148, v149\n"
      "v_exp_f32 v152, v152\n"
      "v_exp_f32 v153, v153\n"
      "v_exp_f32 v154, v154\n"
      "v_add_f32 v215, v150, v215\n"
      "v_add_f32 v216, v151, v216\n"
      "v_cvt_pk_bf16_f32 v147, v150, v151\n"
      "v_exp_f32 v155, v155\n"
      "v_exp_f32 v156, v156\n"
      "v_exp_f32 v157, v157\n"
      "v_add_f32 v215, v152, v215\n"
      "v_add_f32 v216, v153, v216\n"
      "v_add_f32 v215, v154, v215\n"
      "v_cvt_pk_bf16_f32 v148, v152, v153\n"
      "v_exp_f32 v158, v158\n"
      "v_exp_f32 v159, v159\n"
      "v_add_f32 v216, v155, v216\n"
      "v_add_f32 v215, v156, v215\n"
      "v_add_f32 v216, v157, v216\n"
      "v_cvt_pk_bf16_f32 v149, v154, v155\n"
      "v_cvt_pk_bf16_f32 v150, v156, v157\n"
      "v_add_f32 v215, v158, v215\n"
      "v_add_f32 v216, v159, v216\n"
      "v_cvt_pk_bf16_f32 v151, v158, v159\n"
      "s_waitcnt lgkmcnt(0)\n"
      "global_load_dwordx4 v[96:99], v211, s[40:41] offset:-4096 nt\n"
      "global_load_dwordx4 v[100:103], v211, s[40:41] offset:-3072 nt\n"
      "global_load_dwordx4 v[104:107], v211, s[40:41] offset:-2048 nt\n"
      "global_load_dwordx4 v[108:111], v211, s[40:41] offset:-1024 nt\n"
      "global_load_dwordx4 v[112:115], v211, s[40:41] offset:0 nt\n"
      "global_load_dwordx4 v[116:119], v211, s[40:41] offset:1024 nt\n"
      "global_load_dwordx4 v[120:123], v211, s[40:41] offset:2048 nt\n"
      "global_load_dwordx4 v[124:127], v211, s[40:41] offset:3072 nt\n"
      "s_barrier\n"
      "v_mov_b32 v217, 159760\n"
      "ds_read_b128 v[176:179], v217\n"
      "s_waitcnt lgkmcnt(0)\n"
      "v_or_b32 v176, v176, v177\n"
      "v_or3_b32 v176, v176, v178, v179\n"
      "v_cmp_ne_u32 vcc, 2, v176\n"
      "s_nop 4\n"
      "s_cmp_lg_u64 vcc, 0\n"
      "s_cselect_b32 s62, 1, 0\n"
      "s_barrier\n"
      "s_cmp_lg_u32 s62, 0\n"
      "s_cbranch_scc1 .Lsp2skip_%=\n"
      "ds_read_b128 v[176:179], v210 offset:0\n"
      "ds_read_b128 v[180:183], v210 offset:1024\n"
      "ds_read_b128 v[184:187], v210 offset:2048\n"
      "ds_read_b128 v[188:191], v210 offset:3072\n"
      "ds_read_b128 v[192:195], v210 offset:4096\n"
      "ds_read_b128 v[196:199], v210 offset:5120\n"
      "ds_read_b128 v[200:203], v210 offset:6144\n"
      "ds_read_b128 v[204:207], v210 offset:7168\n"
      "s_waitcnt lgkmcnt(4)\n"
      "v_mfma_f32_32x32x16_bf16 v[0:15], v[160:163], v[176:179], 0\n"
      "ds_read_b128 v[176:179], v210 offset:8192\n"
      "v_mfma_f32_32x32x16_bf16 v[16:31], v[160:163], v[180:183], 0\n"
      "ds_read_b128 v[180:183], v210 offset:9216\n"
      "v_mfma_f32_32x32x16_bf16 v[32:47], v[160:163], v[184:187], 0\n"
      "ds_read_b128 v[184:187], v210 offset:10240\n"
      "v_mfma_f32_32x32x16_bf16 v[48:63], v[160:163], v[188:191], 0\n"
      "ds_read_b128 v[188:191], v210 offset:11264\n"
      "s_waitcnt lgkmcnt(4)\n"
      "v_mfma_f32_32x32x16_bf16 v[0:15], v[164:167], v[192:195], v[0:15]\n"
      "ds_read_b128 v[192:195], v210 offset:12288\n"
      "v_add_f32 v215, v215, v216\n"
      "v_mov_b32 v216, v215\n"
      "v_mfma_f32_32x32x16_bf16 v[16:31], v[164:167], v[196:199], v[16:31]\n"
      "ds_read_b128 v[196:199], v210 offset:13312\n"
      "v_permlane32_swap_b32 v215, v216\n"
      "v_mfma_f32_32x32x16_bf16 v[32:47], v[164:167], v[200:203], v[32:47]\n"
      "ds_read_b128 v[200:203], v210 offset:14336\n"
      "v_add_f32 v215, v215, v216\n"
      "v_rcp_f32 v217, v215\n"
      "v_cmp_nle_f32 vcc, s56, v215\n"
      "v_cmp_nge_f32 s[66:67], s57, v215\n"
      "v_mfma_f32_32x32x16_bf16 v[48:63], v[164:167], v[204:207], v[48:63]\n"
      "ds_read_b128 v[204:207], v210 offset:15360\n"
      "ds_write_b32 v228, v217\n"
      "s_waitcnt lgkmcnt(5)\n"
      "v_mfma_f32_32x32x16_bf16 v[0:15], v[168:171], v[176:179], v[0:15]\n"
      "ds_read_b128 v[176:179], v210 offset:16384\n"
      "v_mfma_f32_32x32x16_bf16 v[16:31], v[168:171], v[180:183], v[16:31]\n"
      "ds_read_b128 v[180:183], v210 offset:17408\n"
      "v_mfma_f32_32x32x16_bf16 v[32:47], v[168:171], v[184:187], v[32:47]\n"
      "ds_read_b128 v[184:187], v210 offset:18432\n"
      "v_mfma_f32_32x32x16_bf16 v[48:63], v[168:171], v[188:191], v[48:63]\n"
      "ds_read_b128 v[188:191], v210 offset:19456\n"
      "s_waitcnt lgkmcnt(5)\n"
      "v_mfma_f32_32x32x16_bf16 v[0:15], v[172:175], v[192:195], v[0:15]\n"
      "ds_read_b128 v[192:195], v210 offset:20480\n"
      "v_mfma_f32_32x32x16_bf16 v[16:31], v[172:175], v[196:199], v[16:31]\n"
      "ds_read_b128 v[196:199], v210 offset:21504\n"
      "v_mfma_f32_32x32x16_bf16 v[32:47], v[172:175], v[200:203], v[32:47]\n"
      "ds_read_b128 v[200:203], v210 offset:22528\n"
      "v_mfma_f32_32x32x16_bf16 v[48:63], v[172:175], v[204:207], v[48:63]\n"
      "ds_read_b128 v[204:207], v210 offset:23552\n"
      "s_waitcnt lgkmcnt(4)\n"
      "v_mfma_f32_32x32x16_bf16 v[0:15], v[236:239], v[176:179], v[0:15]\n"
      "ds_read_b128 v[176:179], v210 offset:24576\n"
      "v_mfma_f32_32x32x16_bf16 v[16:31], v[236:239], v[180:183], v[16:31]\n"
      "ds_read_b128 v[180:183], v210 offset:25600\n"
      "v_mfma_f32_32x32x16_bf16 v[32:47], v[236:239], v[184:187], v[32:47]\n"
      "ds_read_b128 v[184:187], v210 offset:26624\n"
      "v_mfma_f32_32x32x16_bf16 v[48:63], v[236:239], v[188:191], v[48:63]\n"
      "ds_read_b128 v[188:191], v210 offset:27648\n"
      "s_waitcnt lgkmcnt(4)\n"
      "v_mfma_f32_32x32x16_bf16 v[0:15], v[240:243], v[192:195], v[0:15]\n"
      "ds_read_b128 v[192:195], v210 offset:28672\n"
      "v_mfma_f32_32x32x16_bf16 v[16:31], v[240:243], v[196:199], v[16:31]\n"
      "ds_read_b128 v[196:199], v210 offset:29696\n"
      "v_mfma_f32_32x32x16_bf16 v[32:47], v[240:243], v[200:203], v[32:47]\n"
      "ds_read_b128 v[200:203], v210 offset:30720\n"
      "v_mfma_f32_32x32x16_bf16 v[48:63], v[240:243], v[204:207], v[48:63]\n"
      "ds_read_b128 v[204:207], v210 offset:31744\n"
      "s_waitcnt lgkmcnt(4)\n"
      "v_mfma_f32_32x32x16_bf16 v[0:15], v[244:247], v[176:179], v[0:15]\n"
      "ds_read_b128 v[176:179], v210 offset:32768\n"
      "v_mfma_f32_32x32x16_bf16 v[16:31], v[244:247], v[180:183], v[16:31]\n"
      "ds_read_b128 v[180:183], v210 offset:33792\n"
      "v_mfma_f32_32x32x16_bf16 v[32:47], v[244:247], v[184:187], v[32:47]\n"
      "ds_read_b128 v[184:187], v210 offset:34816\n"
      "v_mfma_f32_32x32x16_bf16 v[48:63], v[244:247], v[188:191], v[48:63]\n"
      "ds_read_b128 v[188:191], v210 offset:35840\n"
      "s_waitcnt lgkmcnt(4)\n"
      "v_mfma_f32_32x32x16_bf16 v[0:15], v[248:251], v[192:195], v[0:15]\n"
      "ds_read_b128 v[192:195], v210 offset:36864\n"
      "v_mfma_f32_32x32x16_bf16 v[16:31], v[248:251], v[196:199], v[16:31]\n"
      "ds_read_b128 v[196:199], v210 offset:37888\n"
      "v_mfma_f32_32x32x16_bf16 v[32:47], v[248:251], v[200:203], v[32:47]\n"
      "ds_read_b128 v[200:203], v210 offset:38912\n"
      "v_mfma_f32_32x32x16_bf16 v[48:63], v[248:251], v[204:207], v[48:63]\n"
      "ds_read_b128 v[204:207], v210 offset:39936\n"
      "s_waitcnt lgkmcnt(4)\n"
      "v_mfma_f32_32x32x16_bf16 v[0:15], v[218:221], v[176:179], v[0:15]\n"
      "ds_read_b128 v[176:179], v210 offset:40960\n"
      "v_mfma_f32_32x32x16_bf16 v[16:31], v[218:221], v[180:183], v[16:31]\n"
      "ds_read_b128 v[180:183], v210 offset:41984\n"
      "v_mfma_f32_32x32x16_bf16 v[32:47], v[218:221], v[184:187], v[32:47]\n"
      "ds_read_b128 v[184:187], v210 offset:43008\n"
      "v_mfma_f32_32x32x16_bf16 v[48:63], v[218:221], v[188:191], v[48:63]\n"
      "ds_read_b128 v[188:191], v210 offset:44032\n"
      "s_waitcnt lgkmcnt(4)\n"
      "v_mfma_f32_32x32x16_bf16 v[0:15], v[222:225], v[192:195], v[0:15]\n"
      "ds_read_b128 v[192:195], v210 offset:45056\n"
      "v_mfma_f32_32x32x16_bf16 v[16:31], v[222:225], v[196:199], v[16:31]\n"
      "ds_read_b128 v[196:199], v210 offset:46080\n"
      "v_mfma_f32_32x32x16_bf16 v[32:47], v[222:225], v[200:203], v[32:47]\n"
      "ds_read_b128 v[200:203], v210 offset:47104\n"
      "v_mfma_f32_32x32x16_bf16 v[48:63], v[222:225], v[204:207], v[48:63]\n"
      "ds_read_b128 v[204:207], v210 offset:48128\n"
      "s_waitcnt lgkmcnt(4)\n"
      "v_mfma_f32_32x32x16_bf16 v[0:15], v[230:233], v[176:179], v[0:15]\n"
      "ds_read_b128 v[176:179], v210 offset:49152\n"
      "v_mfma_f32_32x32x16_bf16 v[16:31], v[230:233], v[180:183], v[16:31]\n"
      "ds_read_b128 v[180:183], v210 offset:50176\n"
      "v_mfma_f32_32x32x16_bf16 v[32:47], v[230:233], v[184:187], v[32:47]\n"
      "ds_read_b128 v[184:187], v210 offset:51200\n"
      "v_mfma_f32_32x32x16_bf16 v[48:63], v[230:233], v[188:191], v[48:63]\n"
      "ds_read_b128 v[188:191], v210 offset:52224\n"
      "s_waitcnt lgkmcnt(4)\n"
      "v_mfma_f32_32x32x16_bf16 v[0:15], v[252:255], v[192:195], v[0:15]\n"
      "ds_read_b128 v[192:195], v210 offset:53248\n"
      "v_mfma_f32_32x32x16_bf16 v[16:31], v[252:255], v[196:199], v[16:31]\n"
      "ds_read_b128 v[196:199], v210 offset:54272\n"
      "v_mfma_f32_32x32x16_bf16 v[32:47], v[252:255], v[200:203], v[32:47]\n"
      "ds_read_b128 v[200:203], v210 offset:55296\n"
      "v_mfma_f32_32x32x16_bf16 v[48:63], v[252:255], v[204:207], v[48:63]\n"
      "ds_read_b128 v[204:207], v210 offset:56320\n"
      "s_waitcnt lgkmcnt(4)\n"
      "v_mfma_f32_32x32x16_bf16 v[0:15], v[128:131], v[176:179], v[0:15]\n"
      "ds_read_b128 v[176:179], v210 offset:57344\n"
      "v_mfma_f32_32x32x16_bf16 v[16:31], v[128:131], v[180:183], v[16:31]\n"
      "ds_read_b128 v[180:183], v210 offset:58368\n"
      "v_mfma_f32_32x32x16_bf16 v[32:47], v[128:131], v[184:187], v[32:47]\n"
      "ds_read_b128 v[184:187], v210 offset:59392\n"
      "v_mfma_f32_32x32x16_bf16 v[48:63], v[128:131], v[188:191], v[48:63]\n"
      "ds_read_b128 v[188:191], v210 offset:60416\n"
      "s_waitcnt lgkmcnt(4)\n"
      "v_mfma_f32_32x32x16_bf16 v[0:15], v[132:135], v[192:195], v[0:15]\n"
      "ds_read_b128 v[192:195], v210 offset:61440\n"
      "v_mfma_f32_32x32x16_bf16 v[16:31], v[132:135], v[196:199], v[16:31]\n"
      "ds_read_b128 v[196:199], v210 offset:62464\n"
      "v_mfma_f32_32x32x16_bf16 v[32:47], v[132:135], v[200:203], v[32:47]\n"
      "ds_read_b128 v[200:203], v210 offset:63488\n"
      "v_mfma_f32_32x32x16_bf16 v[48:63], v[132:135], v[204:207], v[48:63]\n"
      "ds_read_b128 v[204:207], v210 offset:64512\n"
      "s_waitcnt lgkmcnt(4)\n"
      "v_mfma_f32_32x32x16_bf16 v[0:15], v[144:147], v[176:179], v[0:15]\n"
      "v_mfma_f32_32x32x16_bf16 v[16:31], v[144:147], v[180:183], v[16:31]\n"
      "v_mfma_f32_32x32x16_bf16 v[32:47], v[144:147], v[184:187], v[32:47]\n"
      "ds_read_b128 v[128:131], v229 offset:0\n"
      "ds_read_b128 v[132:135], v229 offset:32\n"
      "ds_read_b128 v[136:139], v229 offset:64\n"
      "ds_read_b128 v[140:143], v229 offset:96\n"
      "v_mfma_f32_32x32x16_bf16 v[48:63], v[144:147], v[188:191], v[48:63]\n"
      "s_waitcnt lgkmcnt(4)\n"
      "v_mfma_f32_32x32x16_bf16 v[0:15], v[148:151], v[192:195], v[0:15]\n"
      "v_mfma_f32_32x32x16_bf16 v[16:31], v[148:151], v[196:199], v[16:31]\n"
      "v_mfma_f32_32x32x16_bf16 v[32:47], v[148:151], v[200:203], v[32:47]\n"
      "v_mfma_f32_32x32x16_bf16 v[48:63], v[148:151], v[204:207], v[48:63]\n"
      ".Lsp2skip_%=:\n"
      "v_xor_b32 v236, 64, v212\n"
      "v_xor_b32 v237, 128, v212\n"
      "v_xor_b32 v238, 192, v212\n"
      "v_xor_b32 v239, 32, v213\n"
      "v_xor_b32 v240, 64, v213\n"
      "v_xor_b32 v241, 96, v213\n"
      "v_xor_b32 v242, 128, v213\n"
      "v_xor_b32 v243, 160, v213\n"
      "v_xor_b32 v244, 192, v213\n"
      "v_xor_b32 v245, 224, v213\n"
      "s_branch .Lepi_%=\n"
      ".Lblk_%=:\n"
      "ds_read_b128 v[176:179], v208 offset:0\n"
      "ds_read_b128 v[180:183], v209 offset:0\n"
      "ds_read_b128 v[184:187], v208 offset:512\n"
      "ds_read_b128 v[188:191], v209 offset:512\n"
      "ds_read_b128 v[192:195], v208 offset:1024\n"
      "ds_read_b128 v[196:199], v209 offset:1024\n"
      "ds_read_b128 v[200:203], v208 offset:1536\n"
      "ds_read_b128 v[204:207], v209 offset:1536\n"
      "s_waitcnt lgkmcnt(4)\n"
      "v_mfma_f32_32x32x16_bf16 v[128:143], v[176:179], v[64:67], 0\n"
      "ds_read_b128 v[176:179], v208 offset:8192\n"
      "v_mfma_f32_32x32x16_bf16 v[128:143], v[180:183], v[68:71], v[128:143]\n"
      "ds_read_b128 v[180:183], v209 offset:8192\n"
      "v_mfma_f32_32x32x16_bf16 v[128:143], v[184:187], v[72:75], v[128:143]\n"
      "ds_read_b128 v[184:187], v208 offset:8704\n"
      "v_mfma_f32_32x32x16_bf16 v[128:143], v[188:191], v[76:79], v[128:143]\n"
      "ds_read_b128 v[188:191], v209 offset:8704\n"
      "s_waitcnt lgkmcnt(4)\n"
      "v_mfma_f32_32x32x16_bf16 v[128:143], v[192:195], v[80:83], v[128:143]\n"
      "ds_read_b128 v[192:195], v208 offset:9216\n"
      "v_mfma_f32_32x32x16_bf16 v[128:143], v[196:199], v[84:87], v[128:143]\n"
      "ds_read_b128 v[196:199], v209 offset:9216\n"
      "v_mfma_f32_32x32x16_bf16 v[128:143], v[200:203], v[88:91], v[128:143]\n"
      "ds_read_b128 v[200:203], v208 offset:9728\n"
      "v_mfma_f32_32x32x16_bf16 v[128:143], v[204:207], v[92:95], v[128:143]\n"
      "ds_read_b128 v[204:207], v209 offset:9728\n"
      "s_waitcnt lgkmcnt(4)\n"
      "v_mfma_f32_32x32x16_bf16 v[144:159], v[176:179], v[64:67], 0\n"
      "ds_read_b128 v[176:179], v210 offset:0\n"
      "v_mfma_f32_32x32x16_bf16 v[144:159], v[180:183], v[68:71], v[144:159]\n"
      "ds_read_b128 v[180:183], v210 offset:1024\n"
      "v_mfma_f32_32x32x16_bf16 v[144:159], v[184:187], v[72:75], v[144:159]\n"
      "ds_read_b128 v[184:187], v208 offset:16384\n"
      "s_nop 3\n"
      "v_exp_f32 v128, v128\n"
      "v_exp_f32 v129, v129\n"
      "v_exp_f32 v130, v130\n"
      "v_exp_f32 v131, v131\n"
      "v_mfma_f32_32x32x16_bf16 v[144:159], v[188:191], v[76:79], v[144:159]\n"
      "ds_read_b128 v[188:191], v209 offset:16384\n"
      "v_exp_f32 v132, v132\n"
      "v_exp_f32 v133, v133\n"
      "v_exp_f32 v134, v134\n"
      "v_mov_b32 v215, v128\n"
      "v_mov_b32 v216, v129\n"
      "v_add_f32 v215, v130, v215\n"
      "v_add_f32 v216, v131, v216\n"
      "v_cvt_pk_bf16_f32 v160, v128, v129\n"
      "v_cvt_pk_bf16_f32 v161, v130, v131\n"
      "s_waitcnt lgkmcnt(4)\n"
      "v_mfma_f32_32x32x16_bf16 v[144:159], v[192:195], v[80:83], v[144:159]\n"
      "ds_read_b128 v[192:195], v210 offset:2048\n"
      "v_exp_f32 v135, v135\n"
      "v_exp_f32 v136, v136\n"
      "v_exp_f32 v137, v137\n"
      "v_add_f32 v215, v132, v215\n"
      "v_add_f32 v216, v133, v216\n"
      "v_add_f32 v215, v134, v215\n"
      "v_cvt_pk_bf16_f32 v162, v132, v133\n"
      "v_mfma_f32_32x32x16_bf16 v[144:159], v[196:199], v[84:87], v[144:159]\n"
      "ds_read_b128 v[196:199], v208 offset:16896\n"
      "v_exp_f32 v138, v138\n"
      "v_exp_f32 v139, v139\n"
      "v_exp_f32 v140, v140\n"
      "v_add_f32 v216, v135, v216\n"
      "v_add_f32 v215, v136, v215\n"
      "v_add_f32 v216, v137, v216\n"
      "v_cvt_pk_bf16_f32 v163, v134, v135\n"
      "v_cvt_pk_bf16_f32 v164, v136, v137\n"
      "v_mfma_f32_32x32x16_bf16 v[144:159], v[200:203], v[88:91], v[144:159]\n"
      "ds_read_b128 v[200:203], v209 offset:16896\n"
      "v_exp_f32 v141, v141\n"
      "v_exp_f32 v142, v142\n"
      "v_exp_f32 v143, v143\n"
      "v_add_f32 v215, v138, v215\n"
      "v_add_f32 v216, v139, v216\n"
      "v_add_f32 v215, v140, v215\n"
      "v_cvt_pk_bf16_f32 v165, v138, v139\n"
      "v_mfma_f32_32x32x16_bf16 v[144:159], v[204:207], v[92:95], v[144:159]\n"
      "ds_read_b128 v[204:207], v210 offset:3072\n"
      "v_add_f32 v216, v141, v216\n"
      "v_add_f32 v215, v142, v215\n"
      "v_add_f32 v216, v143, v216\n"
      "v_cvt_pk_bf16_f32 v166, v140, v141\n"
      "v_cvt_pk_bf16_f32 v167, v142, v143\n"
      "s_nop 7\n"
      "s_nop 3\n"
      "s_barrier\n"
      "s_cmp_lg_u32 s62, 0\n"
      "s_cbranch_scc1 .Lepi_%=\n"
      "s_setprio 1\n"
      "s_waitcnt lgkmcnt(4)\n"
      "v_mfma_f32_32x32x16_bf16 v[0:15], v[160:163], v[176:179], 0\n"
      "ds_read_b128 v[176:179], v208 offset:17408\n"
      "v_exp_f32 v144, v144\n"
      "v_mfma_f32_32x32x16_bf16 v[16:31], v[160:163], v[180:183], 0\n"
      "ds_read_b128 v[180:183], v209 offset:17408\n"
      "v_exp_f32 v145, v145\n"
      "v_add_f32 v215, v144, v215\n"
      "v_mfma_f32_32x32x16_bf16 v[128:143], v[184:187], v[64:67], 0\n"
      "ds_read_b128 v[184:187], v210 offset:4096\n"
      "v_exp_f32 v146, v146\n"
      "v_add_f32 v216, v145, v216\n"
      "v_cvt_pk_bf16_f32 v168, v144, v145\n"
      "v_mfma_f32_32x32x16_bf16 v[128:143], v[188:191], v[68:71], v[128:143]\n"
      "ds_read_b128 v[188:191], v208 offset:17920\n"
      "v_exp_f32 v147, v147\n"
      "v_add_f32 v215, v146, v215\n"
      "s_waitcnt lgkmcnt(4)\n"
      "v_mfma_f32_32x32x16_bf16 v[32:47], v[160:163], v[192:195], 0\n"
      "ds_read_b128 v[192:195], v209 offset:17920\n"
      "v_exp_f32 v148, v148\n"
      "v_add_f32 v216, v147, v216\n"
      "v_cvt_pk_bf16_f32 v169, v146, v147\n"
      "v_mfma_f32_32x32x16_bf16 v[128:143], v[196:199], v[72:75], v[128:143]\n"
      "ds_read_b128 v[196:199], v210 offset:5120\n"
      "v_exp_f32 v149, v149\n"
      "v_add_f32 v215, v148, v215\n"
      "v_mfma_f32_32x32x16_bf16 v[128:143], v[200:203], v[76:79], v[128:143]\n"
      "ds_read_b128 v[200:203], v210 offset:6144\n"
      "v_exp_f32 v150, v150\n"
      "v_add_f32 v216, v149, v216\n"
      "v_cvt_pk_bf16_f32 v170, v148, v149\n"
      "v_mfma_f32_32x32x16_bf16 v[48:63], v[160:163], v[204:207], 0\n"
      "ds_read_b128 v[204:207], v210 offset:7168\n"
      "v_exp_f32 v151, v151\n"
      "v_add_f32 v215, v150, v215\n"
      "s_waitcnt lgkmcnt(4)\n"
      "v_mfma_f32_32x32x16_bf16 v[128:143], v[176:179], v[80:83], v[128:143]\n"
      "ds_read_b128 v[176:179], v210 offset:8192\n"
      "v_exp_f32 v152, v152\n"
      "v_add_f32 v216, v151, v216\n"
      "v_cvt_pk_bf16_f32 v171, v150, v151\n"
      "v_mfma_f32_32x32x16_bf16 v[128:143], v[180:183], v[84:87], v[128:143]\n"
      "ds_read_b128 v[180:183], v210 offset:9216\n"
      "v_exp_f32 v153, v153\n"
      "v_add_f32 v215, v152, v215\n"
      "v_mfma_f32_32x32x16_bf16 v[0:15], v[164:167], v[184:187], v[0:15]\n"
      "ds_read_b128 v[184:187], v208 offset:24576\n"
      "v_exp_f32 v154, v154\n"
      "v_add_f32 v216, v153, v216\n"
      "v_cvt_pk_bf16_f32 v172, v152, v153\n"
      "v_mfma_f32_32x32x16_bf16 v[128:143], v[188:191], v[88:91], v[128:143]\n"
      "ds_read_b128 v[188:191], v209 offset:24576\n"
      "v_exp_f32 v155, v155\n"
      "v_add_f32 v215, v154, v215\n"
      "s_waitcnt lgkmcnt(4)\n"
      "v_mfma_f32_32x32x16_bf16 v[128:143], v[192:195], v[92:95], v[128:143]\n"
      "ds_read_b128 v[192:195], v210 offset:10240\n"
      "v_exp_f32 v156, v156\n"
      "v_add_f32 v216, v155, v216\n"
      "v_cvt_pk_bf16_f32 v173, v154, v155\n"
      "v_mfma_f32_32x32x16_bf16 v[16:31], v[164:167], v[196:199], v[16:31]\n"
      "ds_read_b128 v[196:199], v208 offset:25088\n"
      "v_exp_f32 v157, v157\n"
      "v_add_f32 v215, v156, v215\n"
      "v_mfma_f32_32x32x16_bf16 v[32:47], v[164:167], v[200:203], v[32:47]\n"
      "ds_read_b128 v[200:203], v209 offset:25088\n"
      "v_exp_f32 v158, v158\n"
      "v_add_f32 v216, v157, v216\n"
      "v_cvt_pk_bf16_f32 v174, v156, v157\n"
      "v_mfma_f32_32x32x16_bf16 v[48:63], v[164:167], v[204:207], v[48:63]\n"
      "ds_read_b128 v[204:207], v210 offset:11264\n"
      "v_exp_f32 v159, v159\n"
      "v_add_f32 v215, v158, v215\n"
      "s_waitcnt lgkmcnt(4)\n"
      "v_mfma_f32_32x32x16_bf16 v[0:15], v[168:171], v[176:179], v[0:15]\n"
      "ds_read_b128 v[176:179], v208 offset:25600\n"
      "v_exp_f32 v128, v128\n"
      "v_add_f32 v216, v159, v216\n"
      "v_cvt_pk_bf16_f32 v175, v158, v159\n"
      "v_mfma_f32_32x32x16_bf16 v[16:31], v[168:171], v[180:183], v[16:31]\n"
      "ds_read_b128 v[180:183], v209 offset:25600\n"
      "v_exp_f32 v129, v129\n"
      "v_add_f32 v215, v128, v215\n"
      "v_mfma_f32_32x32x16_bf16 v[144:159], v[184:187], v[64:67], 0\n"
      "ds_read_b128 v[184:187], v210 offset:12288\n"
      "v_exp_f32 v130, v130\n"
      "v_add_f32 v216, v129, v216\n"
      "v_cvt_pk_bf16_f32 v160, v128, v129\n"
      "v_mfma_f32_32x32x16_bf16 v[144:159], v[188:191], v[68:71], v[144:159]\n"
      "ds_read_b128 v[188:191], v208 offset:26112\n"
      "v_exp_f32 v131, v131\n"
      "v_add_f32 v215, v130, v215\n"
      "s_waitcnt lgkmcnt(4)\n"
      "v_mfma_f32_32x32x16_bf16 v[32:47], v[168:171], v[192:195], v[32:47]\n"
      "ds_read_b128 v[192:195], v209 offset:26112\n"
      "v_exp_f32 v132, v132\n"
      "v_add_f32 v216, v131, v216\n"
      "v_cvt_pk_bf16_f32 v161, v130, v131\n"
      "v_mfma_f32_32x32x16_bf16 v[144:159], v[196:199], v[72:75], v[144:159]\n"
      "ds_read_b128 v[196:199], v210 offset:13312\n"
      "v_exp_f32 v133, v133\n"
      "v_add_f32 v215, v132, v215\n"
      "v_mfma_f32_32x32x16_bf16 v[144:159], v[200:203], v[76:79], v[144:159]\n"
      "ds_read_b128 v[200:203], v210 offset:14336\n"
      "v_exp_f32 v134, v134\n"
      "v_add_f32 v216, v133, v216\n"
      "v_cvt_pk_bf16_f32 v162, v132, v133\n"
      "v_mfma_f32_32x32x16_bf16 v[48:63], v[168:171], v[204:207], v[48:63]\n"
      "ds_read_b128 v[204:207], v210 offset:15360\n"
      "v_exp_f32 v135, v135\n"
      "v_add_f32 v215, v134, v215\n"
      "s_waitcnt lgkmcnt(4)\n"
      "v_mfma_f32_32x32x16_bf16 v[144:159], v[176:179], v[80:83], v[144:159]\n"
      "ds_read_b128 v[176:179], v210 offset:16384\n"
      "v_exp_f32 v136, v136\n"
      "v_add_f32 v216, v135, v216\n"
      "v_cvt_pk_bf16_f32 v163, v134, v135\n"
      "v_mfma_f32_32x32x16_bf16 v[144:159], v[180:183], v[84:87], v[144:159]\n"
      "ds_read_b128 v[180:183], v210 offset:17408\n"
      "v_exp_f32 v137, v137\n"
      "v_add_f32 v215, v136, v215\n"
      "v_mfma_f32_32x32x16_bf16 v[0:15], v[172:175], v[184:187], v[0:15]\n"
      "ds_read_b128 v[184:187], v208 offset:32768\n"
      "v_exp_f32 v138, v138\n"
      "v_add_f32 v216, v137, v216\n"
      "v_cvt_pk_bf16_f32 v164, v136, v137\n"
      "v_mfma_f32_32x32x16_bf16 v[144:159], v[188:191], v[88:91], v[144:159]\n"
      "ds_read_b128 v[188:191], v209 offset:32768\n"
      "v_exp_f32 v139, v139\n"
      "v_add_f32 v215, v138, v215\n"
      "s_waitcnt lgkmcnt(4)\n"
      "v_mfma_f32_32x32x16_bf16 v[144:159], v[192:195], v[92:95], v[144:159]\n"
      "ds_read_b128 v[192:195], v210 offset:18432\n"
      "v_exp_f32 v140, v140\n"
      "v_add_f32 v216, v139, v216\n"
      "v_cvt_pk_bf16_f32 v165, v138, v139\n"
      "v_mfma_f32_32x32x16_bf16 v[16:31], v[172:175], v[196:199], v[16:31]\n"
      "ds_read_b128 v[196:199], v208 offset:33280\n"
      "v_exp_f32 v141, v141\n"
      "v_add_f32 v215, v140, v215\n"
      "v_mfma_f32_32x32x16_bf16 v[32:47], v[172:175], v[200:203], v[32:47]\n"
      "ds_read_b128 v[200:203], v209 offset:33280\n"
      "v_exp_f32 v142, v142\n"
      "v_add_f32 v216, v141, v216\n"
      "v_cvt_pk_bf16_f32 v166, v140, v141\n"
      "v_mfma_f32_32x32x16_bf16 v[48:63], v[172:175], v[204:207], v[48:63]\n"
      "ds_read_b128 v[204:207], v210 offset:19456\n"
      "v_exp_f32 v143, v143\n"
      "v_add_f32 v215, v142, v215\n"
      "s_waitcnt lgkmcnt(4)\n"
      "v_mfma_f32_32x32x16_bf16 v[0:15], v[160:163], v[176:179], v[0:15]\n"
      "ds_read_b128 v[176:179], v208 offset:33792\n"
      "v_exp_f32 v144, v144\n"
      "v_add_f32 v216, v143, v216\n"
      "v_cvt_pk_bf16_f32 v167, v142, v143\n"
      "v_mfma_f32_32x32x16_bf16 v[16:31], v[160:163], v[180:183], v[16:31]\n"
      "ds_read_b128 v[180:183], v209 offset:33792\n"
      "v_exp_f32 v145, v145\n"
      "v_add_f32 v215, v144, v215\n"
      "v_mfma_f32_32x32x16_bf16 v[128:143], v[184:187], v[64:67], 0\n"
      "ds_read_b128 v[184:187], v210 offset:20480\n"
      "v_exp_f32 v146, v146\n"
      "v_add_f32 v216, v145, v216\n"
      "v_cvt_pk_bf16_f32 v168, v144, v145\n"
      "v_mfma_f32_32x32x16_bf16 v[128:143], v[188:191], v[68:71], v[128:143]\n"
      "ds_read_b128 v[188:191], v208 offset:34304\n"
      "v_exp_f32 v147, v147\n"
      "v_add_f32 v215, v146, v215\n"
      "s_waitcnt lgkmcnt(4)\n"
      "v_mfma_f32_32x32x16_bf16 v[32:47], v[160:163], v[192:195], v[32:47]\n"
      "ds_read_b128 v[192:195], v209 offset:34304\n"
      "v_exp_f32 v148, v148\n"
      "v_add_f32 v216, v147, v216\n"
      "v_cvt_pk_bf16_f32 v169, v146, v147\n"
      "v_mfma_f32_32x32x16_bf16 v[128:143], v[196:199], v[72:75], v[128:143]\n"
      "ds_read_b128 v[196:199], v210 offset:21504\n"
      "v_exp_f32 v149, v149\n"
      "v_add_f32 v215, v148, v215\n"
      "v_mfma_f32_32x32x16_bf16 v[128:143], v[200:203], v[76:79], v[128:143]\n"
      "ds_read_b128 v[200:203], v210 offset:22528\n"
      "v_exp_f32 v150, v150\n"
      "v_add_f32 v216, v149, v216\n"
      "v_cvt_pk_bf16_f32 v170, v148, v149\n"
      "v_mfma_f32_32x32x16_bf16 v[48:63], v[160:163], v[204:207], v[48:63]\n"
      "ds_read_b128 v[204:207], v210 offset:23552\n"
      "v_exp_f32 v151, v151\n"
      "v_add_f32 v215, v150, v215\n"
      "s_waitcnt lgkmcnt(4)\n"
      "v_mfma_f32_32x32x16_bf16 v[128:143], v[176:179], v[80:83], v[128:143]\n"
      "ds_read_b128 v[176:179], v210 offset:24576\n"
      "v_exp_f32 v152, v152\n"
      "v_add_f32 v216, v151, v216\n"
      "v_cvt_pk_bf16_f32 v171, v150, v151\n"
      "v_mfma_f32_32x32x16_bf16 v[128:143], v[180:183], v[84:87], v[128:143]\n"
      "ds_read_b128 v[180:183], v210 offset:25600\n"
      "v_exp_f32 v153, v153\n"
      "v_add_f32 v215, v152, v215\n"
      "v_mfma_f32_32x32x16_bf16 v[0:15], v[164:167], v[184:187], v[0:15]\n"
      "ds_read_b128 v[184:187], v208 offset:40960\n"
      "v_exp_f32 v154, v154\n"
      "v_add_f32 v216, v153, v216\n"
      "v_cvt_pk_bf16_f32 v172, v152, v153\n"
      "v_mfma_f32_32x32x16_bf16 v[128:143], v[188:191], v[88:91], v[128:143]\n"
      "ds_read_b128 v[188:191], v209 offset:40960\n"
      "v_exp_f32 v155, v155\n"
      "v_add_f32 v215, v154, v215\n"
      "s_waitcnt lgkmcnt(4)\n"
      "v_mfma_f32_32x32x16_bf16 v[128:143], v[192:195], v[92:95], v[128:143]\n"
      "ds_read_b128 v[192:195], v210 offset:26624\n"
      "v_exp_f32 v156, v156\n"
      "v_add_f32 v216, v155, v216\n"
      "v_cvt_pk_bf16_f32 v173, v154, v155\n"
      "v_mfma_f32_32x32x16_bf16 v[16:31], v[164:167], v[196:199], v[16:31]\n"
      "ds_read_b128 v[196:199], v208 offset:41472\n"
      "v_exp_f32 v157, v157\n"
      "v_add_f32 v215, v156, v215\n"
      "v_mfma_f32_32x32x16_bf16 v[32:47], v[164:167], v[200:203], v[32:47]\n"
      "ds_read_b128 v[200:203], v209 offset:41472\n"
      "v_exp_f32 v158, v158\n"
      "v_add_f32 v216, v157, v216\n"
      "v_cvt_pk_bf16_f32 v174, v156, v157\n"
      "v_mfma_f32_32x32x16_bf16 v[48:63], v[164:167], v[204:207], v[48:63]\n"
      "ds_read_b128 v[204:207], v210 offset:27648\n"
      "v_exp_f32 v159, v159\n"
      "v_add_f32 v215, v158, v215\n"
      "s_waitcnt lgkmcnt(4)\n"
      "v_mfma_f32_32x32x16_bf16 v[0:15], v[168:171], v[176:179], v[0:15]\n"
      "ds_read_b128 v[176:179], v208 offset:41984\n"
      "v_exp_f32 v128, v128\n"
      "v_add_f32 v216, v159, v216\n"
      "v_cvt_pk_bf16_f32 v175, v158, v159\n"
      "v_mfma_f32_32x32x16_bf16 v[16:31], v[168:171], v[180:183], v[16:31]\n"
      "ds_read_b128 v[180:183], v209 offset:41984\n"
      "v_exp_f32 v129, v129\n"
      "v_add_f32 v215, v128, v215\n"
      "v_mfma_f32_32x32x16_bf16 v[144:159], v[184:187], v[64:67], 0\n"
      "ds_read_b128 v[184:187], v210 offset:28672\n"
      "v_exp_f32 v130, v130\n"
      "v_add_f32 v216, v129, v216\n"
      "v_cvt_pk_bf16_f32 v160, v128, v129\n"
      "v_mfma_f32_32x32x16_bf16 v[144:159], v[188:191], v[68:71], v[144:159]\n"
      "ds_read_b128 v[188:191], v208 offset:42496\n"
      "v_exp_f32 v131, v131\n"
      "v_add_f32 v215, v130, v215\n"
      "s_waitcnt lgkmcnt(4)\n"
      "v_mfma_f32_32x32x16_bf16 v[32:47], v[168:171], v[192:195], v[32:47]\n"
      "ds_read_b128 v[192:195], v209 offset:42496\n"
      "v_exp_f32 v132, v132\n"
      "v_add_f32 v216, v131, v216\n"
      "v_cvt_pk_bf16_f32 v161, v130, v131\n"
      "v_mfma_f32_32x32x16_bf16 v[144:159], v[196:199], v[72:75], v[144:159]\n"
      "ds_read_b128 v[196:199], v210 offset:29696\n"
      "v_exp_f32 v133, v133\n"
      "v_add_f32 v215, v132, v215\n"
      "v_mfma_f32_32x32x16_bf16 v[144:159], v[200:203], v[76:79], v[144:159]\n"
      "ds_read_b128 v[200:203], v210 offset:30720\n"
      "v_exp_f32 v134, v134\n"
      "v_add_f32 v216, v133, v216\n"
      "v_cvt_pk_bf16_f32 v162, v132, v133\n"
      "v_mfma_f32_32x32x16_bf16 v[48:63], v[168:171], v[204:207], v[48:63]\n"
      "ds_read_b128 v[204:207], v210 offset:31744\n"
      "v_exp_f32 v135, v135\n"
      "v_add_f32 v215, v134, v215\n"
      "s_waitcnt lgkmcnt(4)\n"
      "v_mfma_f32_32x32x16_bf16 v[144:159], v[176:179], v[80:83], v[144:159]\n"
      "ds_read_b128 v[176:179], v210 offset:32768\n"
      "v_exp_f32 v136, v136\n"
      "v_add_f32 v216, v135, v216\n"
      "v_cvt_pk_bf16_f32 v163, v134, v135\n"
      "v_mfma_f32_32x32x16_bf16 v[144:159], v[180:183], v[84:87], v[144:159]\n"
      "ds_read_b128 v[180:183], v210 offset:33792\n"
      "v_exp_f32 v137, v137\n"
      "v_add_f32 v215, v136, v215\n"
      "v_mfma_f32_32x32x16_bf16 v[0:15], v[172:175], v[184:187], v[0:15]\n"
      "ds_read_b128 v[184:187], v208 offset:49152\n"
      "v_exp_f32 v138, v138\n"
      "v_add_f32 v216, v137, v216\n"
      "v_cvt_pk_bf16_f32 v164, v136, v137\n"
      "v_mfma_f32_32x32x16_bf16 v[144:159], v[188:191], v[88:91], v[144:159]\n"
      "ds_read_b128 v[188:191], v209 offset:49152\n"
      "v_exp_f32 v139, v139\n"
      "v_add_f32 v215, v138, v215\n"
      "s_waitcnt lgkmcnt(4)\n"
      "v_mfma_f32_32x32x16_bf16 v[144:159], v[192:195], v[92:95], v[144:159]\n"
      "ds_read_b128 v[192:195], v210 offset:34816\n"
      "v_exp_f32 v140, v140\n"
      "v_add_f32 v216, v139, v216\n"
      "v_cvt_pk_bf16_f32 v165, v138, v139\n"
      "v_mfma_f32_32x32x16_bf16 v[16:31], v[172:175], v[196:199], v[16:31]\n"
      "ds_read_b128 v[196:199], v208 offset:49664\n"
      "v_exp_f32 v141, v141\n"
      "v_add_f32 v215, v140, v215\n"
      "v_mfma_f32_32x32x16_bf16 v[32:47], v[172:175], v[200:203], v[32:47]\n"
      "ds_read_b128 v[200:203], v209 offset:49664\n"
      "v_exp_f32 v142, v142\n"
      "v_add_f32 v216, v141, v216\n"
      "v_cvt_pk_bf16_f32 v166, v140, v141\n"
      "v_mfma_f32_32x32x16_bf16 v[48:63], v[172:175], v[204:207], v[48:63]\n"
      "ds_read_b128 v[204:207], v210 offset:35840\n"
      "v_exp_f32 v143, v143\n"
      "v_add_f32 v215, v142, v215\n"
      "s_waitcnt lgkmcnt(4)\n"
      "v_mfma_f32_32x32x16_bf16 v[0:15], v[160:163], v[176:179], v[0:15]\n"
      "ds_read_b128 v[176:179], v208 offset:50176\n"
      "v_exp_f32 v144, v144\n"
      "v_add_f32 v216, v143, v216\n"
      "v_cvt_pk_bf16_f32 v167, v142, v143\n"
      "v_mfma_f32_32x32x16_bf16 v[16:31], v[160:163], v[180:183], v[16:31]\n"
      "ds_read_b128 v[180:183], v209 offset:50176\n"
      "v_exp_f32 v145, v145\n"
      "v_add_f32 v215, v144, v215\n"
      "v_mfma_f32_32x32x16_bf16 v[128:143], v[184:187], v[64:67], 0\n"
      "ds_read_b128 v[184:187], v210 offset:36864\n"
      "v_exp_f32 v146, v146\n"
      "v_add_f32 v216, v145, v216\n"
      "v_cvt_pk_bf16_f32 v168, v144, v145\n"
      "v_mfma_f32_32x32x16_bf16 v[128:143], v[188:191], v[68:71], v[128:143]\n"
      "ds_read_b128 v[188:191], v208 offset:50688\n"
      "v_exp_f32 v147, v147\n"
      "v_add_f32 v215, v146, v215\n"
      "s_waitcnt lgkmcnt(4)\n"
      "v_mfma_f32_32x32x16_bf16 v[32:47], v[160:163], v[192:195], v[32:47]\n"
      "ds_read_b128 v[192:195], v209 offset:50688\n"
      "v_exp_f32 v148, v148\n"
      "v_add_f32 v216, v147, v216\n"
      "v_cvt_pk_bf16_f32 v169, v146, v147\n"
      "v_mfma_f32_32x32x16_bf16 v[128:143], v[196:199], v[72:75], v[128:143]\n"
      "ds_read_b128 v[196:199], v210 offset:37888\n"
      "v_exp_f32 v149, v149\n"
      "v_add_f32 v215, v148, v215\n"
      "v_mfma_f32_32x32x16_bf16 v[128:143], v[200:203], v[76:79], v[128:143]\n"
      "ds_read_b128 v[200:203], v210 offset:38912\n"
      "v_exp_f32 v150, v150\n"
      "v_add_f32 v216, v149, v216\n"
      "v_cvt_pk_bf16_f32 v170, v148, v149\n"
      "v_mfma_f32_32x32x16_bf16 v[48:63], v[160:163], v[204:207], v[48:63]\n"
      "ds_read_b128 v[204:207], v210 offset:39936\n"
      "v_exp_f32 v151, v151\n"
      "v_add_f32 v215, v150, v215\n"
      "s_waitcnt lgkmcnt(4)\n"
      "v_mfma_f32_32x32x16_bf16 v[128:143], v[176:179], v[80:83], v[128:143]\n"
      "ds_read_b128 v[176:179], v210 offset:40960\n"
      "v_exp_f32 v152, v152\n"
      "v_add_f32 v216, v151, v216\n"
      "v_cvt_pk_bf16_f32 v171, v150, v151\n"
      "v_mfma_f32_32x32x16_bf16 v[128:143], v[180:183], v[84:87], v[128:143]\n"
      "ds_read_b128 v[180:183], v210 offset:41984\n"
      "v_exp_f32 v153, v153\n"
      "v_add_f32 v215, v152, v215\n"
      "v_mfma_f32_32x32x16_bf16 v[0:15], v[164:167], v[184:187], v[0:15]\n"
      "ds_read_b128 v[184:187], v208 offset:57344\n"
      "v_exp_f32 v154, v154\n"
      "v_add_f32 v216, v153, v216\n"
      "v_cvt_pk_bf16_f32 v172, v152, v153\n"
      "v_mfma_f32_32x32x16_bf16 v[128:143], v[188:191], v[88:91], v[128:143]\n"
      "ds_read_b128 v[188:191], v209 offset:57344\n"
      "v_exp_f32 v155, v155\n"
      "v_add_f32 v215, v154, v215\n"
      "s_waitcnt lgkmcnt(4)\n"
      "v_mfma_f32_32x32x16_bf16 v[128:143], v[192:195], v[92:95], v[128:143]\n"
      "ds_read_b128 v[192:195], v210 offset:43008\n"
      "v_exp_f32 v156, v156\n"
      "v_add_f32 v216, v155, v216\n"
      "v_cvt_pk_bf16_f32 v173, v154, v155\n"
      "v_mfma_f32_32x32x16_bf16 v[16:31], v[164:167], v[196:199], v[16:31]\n"
      "ds_read_b128 v[196:199], v208 offset:57856\n"
      "v_exp_f32 v157, v157\n"
      "v_add_f32 v215, v156, v215\n"
      "v_mfma_f32_32x32x16_bf16 v[32:47], v[164:167], v[200:203], v[32:47]\n"
      "ds_read_b128 v[200:203], v209 offset:57856\n"
      "v_exp_f32 v158, v158\n"
      "v_add_f32 v216, v157, v216\n"
      "v_cvt_pk_bf16_f32 v174, v156, v157\n"
      "v_mfma_f32_32x32x16_bf16 v[48:63], v[164:167], v[204:207], v[48:63]\n"
      "ds_read_b128 v[204:207], v210 offset:44032\n"
      "v_exp_f32 v159, v159\n"
      "v_add_f32 v215, v158, v215\n"
      "s_waitcnt lgkmcnt(4)\n"
      "v_mfma_f32_32x32x16_bf16 v[0:15], v[168:171], v[176:179], v[0:15]\n"
      "ds_read_b128 v[176:179], v208 offset:58368\n"
      "v_exp_f32 v128, v128\n"
      "v_add_f32 v216, v159, v216\n"
      "v_cvt_pk_bf16_f32 v175, v158, v159\n"
      "v_mfma_f32_32x32x16_bf16 v[16:31], v[168:171], v[180:183], v[16:31]\n"
      "ds_read_b128 v[180:183], v209 offset:58368\n"
      "v_exp_f32 v129, v129\n"
      "v_add_f32 v215, v128, v215\n"
      "v_mfma_f32_32x32x16_bf16 v[144:159], v[184:187], v[64:67], 0\n"
      "ds_read_b128 v[184:187], v210 offset:45056\n"
      "v_exp_f32 v130, v130\n"
      "v_add_f32 v216, v129, v216\n"
      "v_cvt_pk_bf16_f32 v160, v128, v129\n"
      "v_mfma_f32_32x32x16_bf16 v[144:159], v[188:191], v[68:71], v[144:159]\n"
      "ds_read_b128 v[188:191], v208 offset:58880\n"
      "v_exp_f32 v131, v131\n"
      "v_add_f32 v215, v130, v215\n"
      "s_waitcnt lgkmcnt(4)\n"
      "v_mfma_f32_32x32x16_bf16 v[32:47], v[168:171], v[192:195], v[32:47]\n"
      "ds_read_b128 v[192:195], v209 offset:58880\n"
      "v_exp_f32 v132, v132\n"
      "v_add_f32 v216, v131, v216\n"
      "v_cvt_pk_bf16_f32 v161, v130, v131\n"
      "v_mfma_f32_32x32x16_bf16 v[144:159], v[196:199], v[72:75], v[144:159]\n"
      "ds_read_b128 v[196:199], v210 offset:46080\n"
      "v_exp_f32 v133, v133\n"
      "v_add_f32 v215, v132, v215\n"
      "v_mfma_f32_32x32x16_bf16 v[144:159], v[200:203], v[76:79], v[144:159]\n"
      "ds_read_b128 v[200:203], v210 offset:47104\n"
      "v_exp_f32 v134, v134\n"
      "v_add_f32 v216, v133, v216\n"
      "v_cvt_pk_bf16_f32 v162, v132, v133\n"
      "v_mfma_f32_32x32x16_bf16 v[48:63], v[168:171], v[204:207], v[48:63]\n"
      "ds_read_b128 v[204:207], v210 offset:48128\n"
      "v_exp_f32 v135, v135\n"
      "v_add_f32 v215, v134, v215\n"
      "s_waitcnt lgkmcnt(4)\n"
      "v_mfma_f32_32x32x16_bf16 v[144:159], v[176:179], v[80:83], v[144:159]\n"
      "ds_read_b128 v[176:179], v210 offset:49152\n"
      "v_exp_f32 v136, v136\n"
      "v_add_f32 v216, v135, v216\n"
      "v_cvt_pk_bf16_f32 v163, v134, v135\n"
      "v_mfma_f32_32x32x16_bf16 v[144:159], v[180:183], v[84:87], v[144:159]\n"
      "ds_read_b128 v[180:183], v210 offset:50176\n"
      "v_exp_f32 v137, v137\n"
      "v_add_f32 v215, v136, v215\n"
      "v_mfma_f32_32x32x16_bf16 v[0:15], v[172:175], v[184:187], v[0:15]\n"
      "ds_read_b128 v[184:187], v210 offset:51200\n"
      "v_exp_f32 v138, v138\n"
      "v_add_f32 v216, v137, v216\n"
      "v_cvt_pk_bf16_f32 v164, v136, v137\n"
      "v_mfma_f32_32x32x16_bf16 v[144:159], v[188:191], v[88:91], v[144:159]\n"
      "ds_read_b128 v[188:191], v210 offset:52224\n"
      "v_exp_f32 v139, v139\n"
      "v_add_f32 v215, v138, v215\n"
      "s_waitcnt lgkmcnt(4)\n"
      "v_mfma_f32_32x32x16_bf16 v[144:159], v[192:195], v[92:95], v[144:159]\n"
      "ds_read_b128 v[192:195], v210 offset:53248\n"
      "v_exp_f32 v140, v140\n"
      "v_add_f32 v216, v139, v216\n"
      "v_cvt_pk_bf16_f32 v165, v138, v139\n"
      "v_mfma_f32_32x32x16_bf16 v[16:31], v[172:175], v[196:199], v[16:31]\n"
      "ds_read_b128 v[196:199], v210 offset:54272\n"
      "v_exp_f32 v141, v141\n"
      "v_add_f32 v215, v140, v215\n"
      "v_mfma_f32_32x32x16_bf16 v[32:47], v[172:175], v[200:203], v[32:47]\n"
      "ds_read_b128 v[200:203], v210 offset:55296\n"
      "v_exp_f32 v142, v142\n"
      "v_add_f32 v216, v141, v216\n"
      "v_cvt_pk_bf16_f32 v166, v140, v141\n"
      "v_mfma_f32_32x32x16_bf16 v[48:63], v[172:175], v[204:207], v[48:63]\n"
      "ds_read_b128 v[204:207], v210 offset:56320\n"
      "v_exp_f32 v143, v143\n"
      "v_add_f32 v215, v142, v215\n"
      "s_waitcnt lgkmcnt(4)\n"
      "v_mfma_f32_32x32x16_bf16 v[0:15], v[160:163], v[176:179], v[0:15]\n"
      "ds_read_b128 v[176:179], v210 offset:57344\n"
      "v_exp_f32 v144, v144\n"
      "v_add_f32 v216, v143, v216\n"
      "v_cvt_pk_bf16_f32 v167, v142, v143\n"
      "v_mfma_f32_32x32x16_bf16 v[16:31], v[160:163], v[180:183], v[16:31]\n"
      "ds_read_b128 v[180:183], v210 offset:58368\n"
      "v_exp_f32 v145, v145\n"
      "v_add_f32 v215, v144, v215\n"
      "v_mfma_f32_32x32x16_bf16 v[32:47], v[160:163], v[184:187], v[32:47]\n"
      "ds_read_b128 v[184:187], v210 offset:59392\n"
      "v_exp_f32 v146, v146\n"
      "v_add_f32 v216, v145, v216\n"
      "v_cvt_pk_bf16_f32 v168, v144, v145\n"
      "v_mfma_f32_32x32x16_bf16 v[48:63], v[160:163], v[188:191], v[48:63]\n"
      "ds_read_b128 v[188:191], v210 offset:60416\n"
      "v_exp_f32 v147, v147\n"
      "v_exp_f32 v148, v148\n"
      "v_add_f32 v215, v146, v215\n"
      "s_waitcnt lgkmcnt(4)\n"
      "v_mfma_f32_32x32x16_bf16 v[0:15], v[164:167], v[192:195], v[0:15]\n"
      "ds_read_b128 v[192:195], v210 offset:61440\n"
      "v_exp_f32 v149, v149\n"
      "v_exp_f32 v152, v152\n"
      "v_add_f32 v216, v147, v216\n"
      "v_add_f32 v215, v148, v215\n"
      "v_cvt_pk_bf16_f32 v169, v146, v147\n"
      "v_mfma_f32_32x32x16_bf16 v[16:31], v[164:167], v[196:199], v[16:31]\n"
      "ds_read_b128 v[196:199], v210 offset:62464\n"
      "v_exp_f32 v150, v150\n"
      "v_exp_f32 v153, v153\n"
      "v_add_f32 v216, v149, v216\n"
      "v_add_f32 v215, v152, v215\n"
      "v_cvt_pk_bf16_f32 v170, v148, v149\n"
      "v_mfma_f32_32x32x16_bf16 v[32:47], v[164:167], v[200:203], v[32:47]\n"
      "ds_read_b128 v[200:203], v210 offset:63488\n"
      "v_exp_f32 v151, v151\n"
      "v_exp_f32 v154, v154\n"
      "v_add_f32 v215, v150, v215\n"
      "v_add_f32 v216, v153, v216\n"
      "v_cvt_pk_bf16_f32 v172, v152, v153\n"
      "v_mfma_f32_32x32x16_bf16 v[48:63], v[164:167], v[204:207], v[48:63]\n"
      "ds_read_b128 v[204:207], v210 offset:64512\n"
      "v_exp_f32 v155, v155\n"
      "v_exp_f32 v156, v156\n"
      "v_add_f32 v216, v151, v216\n"
      "v_add_f32 v215, v154, v215\n"
      "v_cvt_pk_bf16_f32 v171, v150, v151\n"
      "s_waitcnt lgkmcnt(4)\n"
      "s_nop 0\n"
      "v_mfma_f32_32x32x16_bf16 v[0:15], v[168:171], v[176:179], v[0:15]\n"
      "v_exp_f32 v157, v157\n"
      "v_add_f32 v216, v155, v216\n"
      "v_add_f32 v215, v156, v215\n"
      "v_cvt_pk_bf16_f32 v173, v154, v155\n"
      "v_mfma_f32_32x32x16_bf16 v[16:31], v[168:171], v[180:183], v[16:31]\n"
      "v_exp_f32 v158, v158\n"
      "v_add_f32 v216, v157, v216\n"
      "v_cvt_pk_bf16_f32 v174, v156, v157\n"
      "v_mfma_f32_32x32x16_bf16 v[32:47], v[168:171], v[184:187], v[32:47]\n"
      "v_exp_f32 v159, v159\n"
      "v_add_f32 v215, v158, v215\n"
      "v_mfma_f32_32x32x16_bf16 v[48:63], v[168:171], v[188:191], v[48:63]\n"
      "v_add_f32 v216, v159, v216\n"
      "v_cvt_pk_bf16_f32 v175, v158, v159\n"
      "s_waitcnt lgkmcnt(0)\n"
      "s_nop 0\n"
      "v_mfma_f32_32x32x16_bf16 v[0:15], v[172:175], v[192:195], v[0:15]\n"
      "v_add_f32 v215, v215, v216\n"
      "v_mov_b32 v216, v215\n"
      "v_mfma_f32_32x32x16_bf16 v[16:31], v[172:175], v[196:199], v[16:31]\n"
      "s_nop 0\n"
      "v_permlane32_swap_b32 v215, v216\n"
      "v_mfma_f32_32x32x16_bf16 v[32:47], v[172:175], v[200:203], v[32:47]\n"
      "v_add_f32 v215, v215, v216\n"
      "v_rcp_f32 v217, v215\n"
      "v_cmp_nle_f32 vcc, s56, v215\n"
      "v_cmp_nge_f32 s[66:67], s57, v215\n"
      "v_mfma_f32_32x32x16_bf16 v[48:63], v[172:175], v[204:207], v[48:63]\n"
      "ds_write_b32 v228, v217\n"
      "ds_read_b128 v[128:131], v229 offset:0\n"
      "ds_read_b128 v[132:135], v229 offset:32\n"
      "ds_read_b128 v[136:139], v229 offset:64\n"
      "ds_read_b128 v[140:143], v229 offset:96\n"
      ".Lepi_%=:\n"
      "s_setprio 0\n"
      "s_cmp_eq_u32 s48, 1\n"
      "s_cbranch_scc0 .Lyb_%=\n"
      "s_cmp_ge_u32 s49, 4\n"
      "s_cbranch_scc1 .Lnyb_%=\n"
      ".Lyb_%=:\n"
      "s_barrier\n"
      ".Lnyb_%=:\n"
      "s_cmp_lg_u32 s62, 0\n"
      "s_cbranch_scc1 .Lslow_%=\n"
      "s_or_b64 vcc, vcc, s[66:67]\n"
      "s_cbranch_scc1 .Lslow_%=\n"
      "s_waitcnt lgkmcnt(0)\n"
      ".Lepi2_%=:\n"
      "s_waitcnt vmcnt(0)\n"
      "s_cmp_eq_u32 s48, 1\n"
      "s_cbranch_scc1 .Lnot0_%=\n"
      "ds_write_b128 v212, v[96:99] offset:0\n"
      "ds_write_b128 v236, v[100:103] offset:1024\n"
      "ds_write_b128 v237, v[104:107] offset:2048\n"
      "ds_write_b128 v238, v[108:111] offset:3072\n"
      "s_mov_b64 exec, s[58:59]\n"
      "ds_read_b128 v[64:67], v213\n"
      "ds_read_b128 v[68:71], v239\n"
      "ds_read_b128 v[72:75], v240\n"
      "ds_read_b128 v[76:79], v241\n"
      "ds_read_b128 v[80:83], v242\n"
      "ds_read_b128 v[84:87], v243\n"
      "ds_read_b128 v[88:91], v244\n"
      "ds_read_b128 v[92:95], v245\n"
      "s_mov_b64 exec, -1\n"
      ".Lnot0_%=:\n"
      "v_mul_f32 v0, v128, v0\n"
      "v_mul_f32 v16, v128, v16\n"
      "v_mul_f32 v32, v128, v32\n"
      "v_mul_f32 v48, v128, v48\n"
      "v_mul_f32 v1, v129, v1\n"
      "v_mul_f32 v17, v129, v17\n"
      "v_mul_f32 v33, v129, v33\n"
      "v_mul_f32 v49, v129, v49\n"
      "v_cvt_pk_bf16_f32 v160, v0, v16\n"
      "v_cvt_pk_bf16_f32 v161, v32, v48\n"
      "v_cvt_pk_bf16_f32 v162, v1, v17\n"
      "v_cvt_pk_bf16_f32 v163, v33, v49\n"
      "v_mul_f32 v2, v130, v2\n"
      "v_mul_f32 v18, v130, v18\n"
      "v_mul_f32 v34, v130, v34\n"
      "v_mul_f32 v50, v130, v50\n"
      "v_mul_f32 v3, v131, v3\n"
      "v_mul_f32 v19, v131, v19\n"
      "v_mul_f32 v35, v131, v35\n"
      "v_mul_f32 v51, v131, v51\n"
      "v_permlane16_swap_b32 v160, v162\n"
      "v_permlane16_swap_b32 v161, v163\n"
      "global_store_dwordx4 v211, v[160:163], s[42:43] offset:-4096 nt\n"
      "v_cvt_pk_bf16_f32 v164, v2, v18\n"
      "v_cvt_pk_bf16_f32 v165, v34, v50\n"
      "v_cvt_pk_bf16_f32 v166, v3, v19\n"
      "v_cvt_pk_bf16_f32 v167, v35, v51\n"
      "v_mul_f32 v4, v132, v4\n"
      "v_mul_f32 v20, v132, v20\n"
      "v_mul_f32 v36, v132, v36\n"
      "v_mul_f32 v52, v132, v52\n"
      "v_mul_f32 v5, v133, v5\n"
      "v_mul_f32 v21, v133, v21\n"
      "v_mul_f32 v37, v133, v37\n"
      "v_mul_f32 v53, v133, v53\n"
      "v_permlane16_swap_b32 v164, v166\n"
      "v_permlane16_swap_b32 v165, v167\n"
      "global_store_dwordx4 v211, v[164:167], s[42:43] offset:-3072 nt\n"
      "v_cvt_pk_bf16_f32 v168, v4, v20\n"
      "v_cvt_pk_bf16_f32 v169, v36, v52\n"
      "v_cvt_pk_bf16_f32 v170, v5, v21\n"
      "v_cvt_pk_bf16_f32 v171, v37, v53\n"
      "v_mul_f32 v6, v134, v6\n"
      "v_mul_f32 v22, v134, v22\n"
      "v_mul_f32 v38, v134, v38\n"
      "v_mul_f32 v54, v134, v54\n"
      "v_mul_f32 v7, v135, v7\n"
      "v_mul_f32 v23, v135, v23\n"
      "v_mul_f32 v39, v135, v39\n"
      "v_mul_f32 v55, v135, v55\n"
      "v_permlane16_swap_b32 v168, v170\n"
      "v_permlane16_swap_b32 v169, v171\n"
      "global_store_dwordx4 v211, v[168:171], s[42:43] offset:-2048 nt\n"
      "v_cvt_pk_bf16_f32 v172, v6, v22\n"
      "v_cvt_pk_bf16_f32 v173, v38, v54\n"
      "v_cvt_pk_bf16_f32 v174, v7, v23\n"
      "v_cvt_pk_bf16_f32 v175, v39, v55\n"
      "v_mul_f32 v8, v136, v8\n"
      "v_mul_f32 v24, v136, v24\n"
      "v_mul_f32 v40, v136, v40\n"
      "v_mul_f32 v56, v136, v56\n"
      "v_mul_f32 v9, v137, v9\n"
      "v_mul_f32 v25, v137, v25\n"
      "v_mul_f32 v41, v137, v41\n"
      "v_mul_f32 v57, v137, v57\n"
      "v_permlane16_swap_b32 v172, v174\n"
      "v_permlane16_swap_b32 v173, v175\n"
      "global_store_dwordx4 v211, v[172:175], s[42:43] offset:-1024 nt\n"
      "s_cmp_eq_u32 s48, 1\n"
      "s_cbranch_scc1 .Lnot1_%=\n"
      "ds_write_b128 v212, v[112:115] offset:0\n"
      "ds_write_b128 v236, v[116:119] offset:1024\n"
      "ds_write_b128 v237, v[120:123] offset:2048\n"
      "ds_write_b128 v238, v[124:127] offset:3072\n"
      "s_mov_b64 exec, s[60:61]\n"
      "ds_read_b128 v[64:67], v213\n"
      "ds_read_b128 v[68:71], v239\n"
      "ds_read_b128 v[72:75], v240\n"
      "ds_read_b128 v[76:79], v241\n"
      "ds_read_b128 v[80:83], v242\n"
      "ds_read_b128 v[84:87], v243\n"
      "ds_read_b128 v[88:91], v244\n"
      "ds_read_b128 v[92:95], v245\n"
      "s_mov_b64 exec, -1\n"
      ".Lnot1_%=:\n"
      "v_cvt_pk_bf16_f32 v160, v8, v24\n"
      "v_cvt_pk_bf16_f32 v161, v40, v56\n"
      "v_cvt_pk_bf16_f32 v162, v9, v25\n"
      "v_cvt_pk_bf16_f32 v163, v41, v57\n"
      "v_mul_f32 v10, v138, v10\n"
      "v_mul_f32 v26, v138, v26\n"
      "v_mul_f32 v42, v138, v42\n"
      "v_mul_f32 v58, v138, v58\n"
      "v_mul_f32 v11, v139, v11\n"
      "v_mul_f32 v27, v139, v27\n"
      "v_mul_f32 v43, v139, v43\n"
      "v_mul_f32 v59, v139, v59\n"
      "v_permlane16_swap_b32 v160, v162\n"
      "v_permlane16_swap_b32 v161, v163\n"
      "global_store_dwordx4 v211, v[160:163], s[42:43] offset:0 nt\n"
      "v_cvt_pk_bf16_f32 v164, v10, v26\n"
      "v_cvt_pk_bf16_f32 v165, v42, v58\n"
      "v_cvt_pk_bf16_f32 v166, v11, v27\n"
      "v_cvt_pk_bf16_f32 v167, v43, v59\n"
      "v_mul_f32 v12, v140, v12\n"
      "v_mul_f32 v28, v140, v28\n"
      "v_mul_f32 v44, v140, v44\n"
      "v_mul_f32 v60, v140, v60\n"
      "v_mul_f32 v13, v141, v13\n"
      "v_mul_f32 v29, v141, v29\n"
      "v_mul_f32 v45, v141, v45\n"
      "v_mul_f32 v61, v141, v61\n"
      "v_permlane16_swap_b32 v164, v166\n"
      "v_permlane16_swap_b32 v165, v167\n"
      "global_store_dwordx4 v211, v[164:167], s[42:43] offset:1024 nt\n"
      "v_cvt_pk_bf16_f32 v168, v12, v28\n"
      "v_cvt_pk_bf16_f32 v169, v44, v60\n"
      "v_cvt_pk_bf16_f32 v170, v13, v29\n"
      "v_cvt_pk_bf16_f32 v171, v45, v61\n"
      "v_mul_f32 v14, v142, v14\n"
      "v_mul_f32 v30, v142, v30\n"
      "v_mul_f32 v46, v142, v46\n"
      "v_mul_f32 v62, v142, v62\n"
      "v_mul_f32 v15, v143, v15\n"
      "v_mul_f32 v31, v143, v31\n"
      "v_mul_f32 v47, v143, v47\n"
      "v_mul_f32 v63, v143, v63\n"
      "v_permlane16_swap_b32 v168, v170\n"
      "v_permlane16_swap_b32 v169, v171\n"
      "global_store_dwordx4 v211, v[168:171], s[42:43] offset:2048 nt\n"
      "v_cvt_pk_bf16_f32 v172, v14, v30\n"
      "v_cvt_pk_bf16_f32 v173, v46, v62\n"
      "v_cvt_pk_bf16_f32 v174, v15, v31\n"
      "v_cvt_pk_bf16_f32 v175, v47, v63\n"
      "s_nop 0\n"
      "v_permlane16_swap_b32 v172, v174\n"
      "v_permlane16_swap_b32 v173, v175\n"
      "global_store_dwordx4 v211, v[172:175], s[42:43] offset:3072 nt\n"
      "s_waitcnt lgkmcnt(0)\n"
      "s_cmp_lt_u32 s48, 3\n"
      "s_cbranch_scc1 .Lnoq2_%=\n"
      "s_add_u32 s40, s40, 0x10000\n"
      "s_addc_u32 s41, s41, 0\n"
      "global_load_dwordx4 v[96:99], v211, s[40:41] offset:-4096 nt\n"
      "global_load_dwordx4 v[100:103], v211, s[40:41] offset:-3072 nt\n"
      "global_load_dwordx4 v[104:107], v211, s[40:41] offset:-2048 nt\n"
      "global_load_dwordx4 v[108:111], v211, s[40:41] offset:-1024 nt\n"
      "global_load_dwordx4 v[112:115], v211, s[40:41] offset:0 nt\n"
      "global_load_dwordx4 v[116:119], v211, s[40:41] offset:1024 nt\n"
      "global_load_dwordx4 v[120:123], v211, s[40:41] offset:2048 nt\n"
      "global_load_dwordx4 v[124:127], v211, s[40:41] offset:3072 nt\n"
      ".Lnoq2_%=:\n"
      "s_add_u32 s42, s42, 0x10000\n"
      "s_addc_u32 s43, s43, 0\n"
      "s_sub_u32 s48, s48, 1\n"
      "s_cmp_lg_u32 s48, 0\n"
      "s_cbranch_scc1 .Lblk_%=\n"
      "s_branch .Lend_%=\n"
      ".Lslow_%=:\n"
      "s_waitcnt lgkmcnt(0)\n"
      "s_nop 15\n"
      "s_nop 15\n"
      "v_mov_b32 v230, 0xff800000\n"
      "v_mov_b32 v215, 0\n"
      "v_mov_b32 v0, 0\n"
      "v_mov_b32 v1, 0\n"
      "v_mov_b32 v2, 0\n"
      "v_mov_b32 v3, 0\n"
      "v_mov_b32 v4, 0\n"
      "v_mov_b32 v5, 0\n"
      "v_mov_b32 v6, 0\n"
      "v_mov_b32 v7, 0\n"
      "v_mov_b32 v8, 0\n"
      "v_mov_b32 v9, 0\n"
      "v_mov_b32 v10, 0\n"
      "v_mov_b32 v11, 0\n"
      "v_mov_b32 v12, 0\n"
      "v_mov_b32 v13, 0\n"
      "v_mov_b32 v14, 0\n"
      "v_mov_b32 v15, 0\n"
      "v_mov_b32 v16, 0\n"
      "v_mov_b32 v17, 0\n"
      "v_mov_b32 v18, 0\n"
      "v_mov_b32 v19, 0\n"
      "v_mov_b32 v20, 0\n"
      "v_mov_b32 v21, 0\n"
      "v_mov_b32 v22, 0\n"
      "v_mov_b32 v23, 0\n"
      "v_mov_b32 v24, 0\n"
      "v_mov_b32 v25, 0\n"
      "v_mov_b32 v26, 0\n"
      "v_mov_b32 v27, 0\n"
      "v_mov_b32 v28, 0\n"
      "v_mov_b32 v29, 0\n"
      "v_mov_b32 v30, 0\n"
      "v_mov_b32 v31, 0\n"
      "v_mov_b32 v32, 0\n"
      "v_mov_b32 v33, 0\n"
      "v_mov_b32 v34, 0\n"
      "v_mov_b32 v35, 0\n"
      "v_mov_b32 v36, 0\n"
      "v_mov_b32 v37, 0\n"
      "v_mov_b32 v38, 0\n"
      "v_mov_b32 v39, 0\n"
      "v_mov_b32 v40, 0\n"
      "v_mov_b32 v41, 0\n"
      "v_mov_b32 v42, 0\n"
      "v_mov_b32 v43, 0\n"
      "v_mov_b32 v44, 0\n"
      "v_mov_b32 v45, 0\n"
      "v_mov_b32 v46, 0\n"
      "v_mov_b32 v47, 0\n"
      "v_mov_b32 v48, 0\n"
      "v_mov_b32 v49, 0\n"
      "v_mov_b32 v50, 0\n"
      "v_mov_b32 v51, 0\n"
      "v_mov_b32 v52, 0\n"
      "v_mov_b32 v53, 0\n"
      "v_mov_b32 v54, 0\n"
      "v_mov_b32 v55, 0\n"
      "v_mov_b32 v56, 0\n"
      "v_mov_b32 v57, 0\n"
      "v_mov_b32 v58, 0\n"
      "v_mov_b32 v59, 0\n"
      "v_mov_b32 v60, 0\n"
      "v_mov_b32 v61, 0\n"
      "v_mov_b32 v62, 0\n"
      "v_mov_b32 v63, 0\n"
      "s_mov_b32 s73, 0\n"
      ".Lsp_pass_%=:\n"
      "s_mov_b32 s72, 0\n"
      ".Lsp_kb_%=:\n"
      "s_lshl_b32 s64, s72, 13\n"
      "v_add_u32 v232, s64, v208\n"
      "v_add_u32 v233, s64, v209\n"
      "v_add_u32 v234, s64, v210\n"
      "ds_read_b128 v[176:179], v232 offset:0\n"
      "ds_read_b128 v[180:183], v233 offset:0\n"
      "ds_read_b128 v[184:187], v232 offset:512\n"
      "ds_read_b128 v[188:191], v233 offset:512\n"
      "ds_read_b128 v[192:195], v232 offset:1024\n"
      "ds_read_b128 v[196:199], v233 offset:1024\n"
      "ds_read_b128 v[200:203], v232 offset:1536\n"
      "ds_read_b128 v[204:207], v233 offset:1536\n"
      "s_waitcnt lgkmcnt(0)\n"
      "v_mfma_f32_32x32x16_bf16 v[144:159], v[176:179], v[64:67], 0\n"
      "v_mfma_f32_32x32x16_bf16 v[144:159], v[180:183], v[68:71], v[144:159]\n"
      "v_mfma_f32_32x32x16_bf16 v[144:159], v[184:187], v[72:75], v[144:159]\n"
      "v_mfma_f32_32x32x16_bf16 v[144:159], v[188:191], v[76:79], v[144:159]\n"
      "v_mfma_f32_32x32x16_bf16 v[144:159], v[192:195], v[80:83], v[144:159]\n"
      "v_mfma_f32_32x32x16_bf16 v[144:159], v[196:199], v[84:87], v[144:159]\n"
      "v_mfma_f32_32x32x16_bf16 v[144:159], v[200:203], v[88:91], v[144:159]\n"
      "v_mfma_f32_32x32x16_bf16 v[144:159], v[204:207], v[92:95], v[144:159]\n"
      "s_nop 15\n"
      "s_cmp_lg_u32 s73, 0\n"
      "s_cbranch_scc1 .Lsp_n0_%=\n"
      "v_max_f32 v230, v230, v144\n"
      "v_max_f32 v230, v230, v145\n"
      "v_max_f32 v230, v230, v146\n"
      "v_max_f32 v230, v230, v147\n"
      "v_max_f32 v230, v230, v148\n"
      "v_max_f32 v230, v230, v149\n"
      "v_max_f32 v230, v230, v150\n"
      "v_max_f32 v230, v230, v151\n"
      "v_max_f32 v230, v230, v152\n"
      "v_max_f32 v230, v230, v153\n"
      "v_max_f32 v230, v230, v154\n"
      "v_max_f32 v230, v230, v155\n"
      "v_max_f32 v230, v230, v156\n"
      "v_max_f32 v230, v230, v157\n"
      "v_max_f32 v230, v230, v158\n"
      "v_max_f32 v230, v230, v159\n"
      "s_branch .Lsp_next_%=\n"
      ".Lsp_n0_%=:\n"
      "v_sub_f32 v144, v144, v230\n"
      "v_sub_f32 v145, v145, v230\n"
      "v_sub_f32 v146, v146, v230\n"
      "v_sub_f32 v147, v147, v230\n"
      "v_sub_f32 v148, v148, v230\n"
      "v_sub_f32 v149, v149, v230\n"
      "v_sub_f32 v150, v150, v230\n"
      "v_sub_f32 v151, v151, v230\n"
      "v_sub_f32 v152, v152, v230\n"
      "v_sub_f32 v153, v153, v230\n"
      "v_sub_f32 v154, v154, v230\n"
      "v_sub_f32 v155, v155, v230\n"
      "v_sub_f32 v156, v156, v230\n"
      "v_sub_f32 v157, v157, v230\n"
      "v_sub_f32 v158, v158, v230\n"
      "v_sub_f32 v159, v159, v230\n"
      "v_exp_f32 v144, v144\n"
      "v_exp_f32 v145, v145\n"
      "v_exp_f32 v146, v146\n"
      "v_exp_f32 v147, v147\n"
      "v_exp_f32 v148, v148\n"
      "v_exp_f32 v149, v149\n"
      "v_exp_f32 v150, v150\n"
      "v_exp_f32 v151, v151\n"
      "v_exp_f32 v152, v152\n"
      "v_exp_f32 v153, v153\n"
      "v_exp_f32 v154, v154\n"
      "v_exp_f32 v155, v155\n"
      "v_exp_f32 v156, v156\n"
      "v_exp_f32 v157, v157\n"
      "v_exp_f32 v158, v158\n"
      "v_exp_f32 v159, v159\n"
      "s_nop 1\n"
      "s_cmp_lg_u32 s73, 1\n"
      "s_cbranch_scc1 .Lsp_n1_%=\n"
      "v_add_f32 v215, v215, v144\n"
      "v_add_f32 v215, v215, v145\n"
      "v_add_f32 v215, v215, v146\n"
      "v_add_f32 v215, v215, v147\n"
      "v_add_f32 v215, v215, v148\n"
      "v_add_f32 v215, v215, v149\n"
      "v_add_f32 v215, v215, v150\n"
      "v_add_f32 v215, v215, v151\n"
      "v_add_f32 v215, v215, v152\n"
      "v_add_f32 v215, v215, v153\n"
      "v_add_f32 v215, v215, v154\n"
      "v_add_f32 v215, v215, v155\n"
      "v_add_f32 v215, v215, v156\n"
      "v_add_f32 v215, v215, v157\n"
      "v_add_f32 v215, v215, v158\n"
      "v_add_f32 v215, v215, v159\n"
      "s_branch .Lsp_next_%=\n"
      ".Lsp_n1_%=:\n"
      "v_mul_f32 v144, v231, v144\n"
      "v_mul_f32 v145, v231, v145\n"
      "v_mul_f32 v146, v231, v146\n"
      "v_mul_f32 v147, v231, v147\n"
      "v_mul_f32 v148, v231, v148\n"
      "v_mul_f32 v149, v231, v149\n"
      "v_mul_f32 v150, v231, v150\n"
      "v_mul_f32 v151, v231, v151\n"
      "v_mul_f32 v152, v231, v152\n"
      "v_mul_f32 v153, v231, v153\n"
      "v_mul_f32 v154, v231, v154\n"
      "v_mul_f32 v155, v231, v155\n"
      "v_mul_f32 v156, v231, v156\n"
      "v_mul_f32 v157, v231, v157\n"
      "v_mul_f32 v158, v231, v158\n"
      "v_mul_f32 v159, v231, v159\n"
      "v_cvt_pk_bf16_f32 v160, v144, v145\n"
      "v_cvt_pk_bf16_f32 v161, v146, v147\n"
      "v_cvt_pk_bf16_f32 v162, v148, v149\n"
      "v_cvt_pk_bf16_f32 v163, v150, v151\n"
      "v_cvt_pk_bf16_f32 v164, v152, v153\n"
      "v_cvt_pk_bf16_f32 v165, v154, v155\n"
      "v_cvt_pk_bf16_f32 v166, v156, v157\n"
      "v_cvt_pk_bf16_f32 v167, v158, v159\n"
      "ds_read_b128 v[176:179], v234 offset:0\n"
      "ds_read_b128 v[180:183], v234 offset:1024\n"
      "ds_read_b128 v[184:187], v234 offset:2048\n"
      "ds_read_b128 v[188:191], v234 offset:3072\n"
      "ds_read_b128 v[192:195], v234 offset:4096\n"
      "ds_read_b128 v[196:199], v234 offset:5120\n"
      "ds_read_b128 v[200:203], v234 offset:6144\n"
      "ds_read_b128 v[204:207], v234 offset:7168\n"
      "s_waitcnt lgkmcnt(0)\n"
      "v_mfma_f32_32x32x16_bf16 v[0:15], v[160:163], v[176:179], v[0:15]\n"
      "v_mfma_f32_32x32x16_bf16 v[16:31], v[160:163], v[180:183], v[16:31]\n"
      "v_mfma_f32_32x32x16_bf16 v[32:47], v[160:163], v[184:187], v[32:47]\n"
      "v_mfma_f32_32x32x16_bf16 v[48:63], v[160:163], v[188:191], v[48:63]\n"
      "v_mfma_f32_32x32x16_bf16 v[0:15], v[164:167], v[192:195], v[0:15]\n"
      "v_mfma_f32_32x32x16_bf16 v[16:31], v[164:167], v[196:199], v[16:31]\n"
      "v_mfma_f32_32x32x16_bf16 v[32:47], v[164:167], v[200:203], v[32:47]\n"
      "v_mfma_f32_32x32x16_bf16 v[48:63], v[164:167], v[204:207], v[48:63]\n"
      "s_nop 15\n"
      ".Lsp_next_%=:\n"
      "s_add_u32 s72, s72, 1\n"
      "s_cmp_lt_u32 s72, 8\n"
      "s_cbranch_scc1 .Lsp_kb_%=\n"
      "s_cmp_lg_u32 s73, 0\n"
      "s_cbranch_scc1 .Lsp_e0_%=\n"
      "v_mov_b32 v217, v230\n"
      "s_nop 1\n"
      "v_permlane32_swap_b32 v230, v217\n"
      "s_nop 1\n"
      "v_max_f32 v230, v230, v217\n"
      "s_mov_b32 s73, 1\n"
      "s_branch .Lsp_pass_%=\n"
      ".Lsp_e0_%=:\n"
      "s_cmp_lg_u32 s73, 1\n"
      "s_cbranch_scc1 .Lsp_e1_%=\n"
      "v_mov_b32 v217, v215\n"
      "s_nop 1\n"
      "v_permlane32_swap_b32 v215, v217\n"
      "s_nop 1\n"
      "v_add_f32 v215, v215, v217\n"
      "v_rcp_f32 v231, v215\n"
      "s_nop 1\n"
      "s_mov_b32 s73, 2\n"
      "s_branch .Lsp_pass_%=\n"
      ".Lsp_e1_%=:\n"
      "v_mov_b32 v128, 1.0\n"
      "v_mov_b32 v129, 1.0\n"
      "v_mov_b32 v130, 1.0\n"
      "v_mov_b32 v131, 1.0\n"
      "v_mov_b32 v132, 1.0\n"
      "v_mov_b32 v133, 1.0\n"
      "v_mov_b32 v134, 1.0\n"
      "v_mov_b32 v135, 1.0\n"
      "v_mov_b32 v136, 1.0\n"
      "v_mov_b32 v137, 1.0\n"
      "v_mov_b32 v138, 1.0\n"
      "v_mov_b32 v139, 1.0\n"
      "v_mov_b32 v140, 1.0\n"
      "v_mov_b32 v141, 1.0\n"
      "v_mov_b32 v142, 1.0\n"
      "v_mov_b32 v143, 1.0\n"
      "s_branch .Lepi2_%=\n"
      ".Lend_%=:\n"
      :
      : "s"(ka), "s"(wid), "s"(blockIdx.x), "s"(ldsb)
      : "memory", "vcc", "scc", "v0", "v1", "v2", "v3", "v4", "v5", "v6", "v7", "v8", "v9", "v10", "v11", "v12", "v13", "v14", "v15", "v16", "v17", "v18", "v19", "v20", "v21", "v22", "v23", "v24", "v25", "v26", "v27", "v28", "v29", "v30", "v31", "v32", "v33", "v34", "v35", "v36", "v37", "v38", "v39", "v40", "v41", "v42", "v43", "v44", "v45", "v46", "v47", "v48", "v49", "v50", "v51", "v52", "v53", "v54", "v55", "v56", "v57", "v58", "v59", "v60", "v61", "v62", "v63", "v64", "v65", "v66", "v67", "v68", "v69", "v70", "v71", "v72", "v73", "v74", "v75", "v76", "v77", "v78", "v79", "v80", "v81", "v82", "v83", "v84", "v85", "v86", "v87", "v88", "v89", "v90", "v91", "v92", "v93", "v94", "v95", "v96", "v97", "v98", "v99", "v100", "v101", "v102", "v103", "v104", "v105", "v106", "v107", "v108", "v109", "v110", "v111", "v112", "v113", "v114", "v115", "v116", "v117", "v118", "v119", "v120", "v121", "v122", "v123", "v124", "v125", "v126", "v127", "v128", "v129", "v130", "v131", "v132", "v133", "v134", "v135", "v136", "v137", "v138", "v139", "v140", "v141", "v142", "v143", "v144", "v145", "v146", "v147", "v148", "v149", "v150", "v151", "v152", "v153", "v154", "v155", "v156", "v157", "v158", "v159", "v160", "v161", "v162", "v163", "v164", "v165", "v166", "v167", "v168", "v169", "v170", "v171", "v172", "v173", "v174", "v175", "v176", "v177", "v178", "v179", "v180", "v181", "v182", "v183", "v184", "v185", "v186", "v187", "v188", "v189", "v190", "v191", "v192", "v193", "v194", "v195", "v196", "v197", "v198", "v199", "v200", "v201", "v202", "v203", "v204", "v205", "v206", "v207", "v208", "v209", "v210", "v211", "v212", "v213", "v214", "v215", "v216", "v217", "v218", "v219", "v220", "v221", "v222", "v223", "v224", "v225", "v226", "v227", "v228", "v229", "v230", "v231", "v232", "v233", "v234", "v235", "v236", "v237", "v238", "v239", "v240", "v241", "v242", "v243", "v244", "v245", "v246", "v247", "v248", "v249", "v250", "v251", "v252", "v253", "v254", "v255", "s38", "s39", "s40", "s41", "s42", "s43", "s44", "s45", "s46", "s47", "s48", "s49", "s50", "s51", "s52", "s53", "s54", "s55", "s56", "s57", "s58", "s59", "s60", "s61", "s62", "s63", "s64", "s65", "s66", "s67", "s68", "s69", "s70", "s71", "s72", "s73", "s74", "s75", "s76", "s77", "s78", "s79", "s80", "s81", "s82", "s83", "s84", "s85", "s86", "s87", "s88", "s89", "s90", "s91", "s92", "s93", "s94", "s95", "s96", "s97", "s98", "s99");
}

extern "C" void kernel_launch(void* const* d_in, const int* in_sizes, int n_in, void* d_out, int out_size, void* d_ws, size_t ws_size,
                              hipStream_t stream) {
  gqa_attn_kernel<<<256, 512, 0, stream>>>((const char*)d_in[0], (const char*)d_in[1], (const char*)d_in[2], (char*)d_out);
}
```
